```python
import torch

def _softmax(x, axis=-1):
    return torch.softmax(x, dim=axis)
def _gelu(x):
    return torch.nn.functional.gelu(x, approximate="tanh")
def _silu(x):
    return torch.nn.functional.silu(x)
def _sigmoid(x):
    return torch.sigmoid(x)
def _layernorm(x, axis=-1, eps=1e-5):
    m = x.mean(dim=axis, keepdim=True)
    v = x.var(dim=axis, keepdim=True, unbiased=False)
    return (x - m) / torch.sqrt(v + eps)
def _scatter_add(tgt, idx, src, axis=0):
    return tgt.clone().index_add_(axis, idx, src)
def _segment_sum(src, seg, n):
    out = torch.zeros((n,) + src.shape[1:], dtype=src.dtype)
    return out.index_add_(0, seg, src)
def _pad(x, pw):
    flat = tuple(v for pair in reversed(pw) for v in pair)
    return torch.nn.functional.pad(x, flat)

kernel_name = 'interleave_layers__sliding_local_dilated__geglu_ffn__prompt'

def setup_inputs():

    _w = (lambda a: (lambda t: t.to(torch.bfloat16) if t.is_floating_point() else t)(torch.as_tensor(a)))
    def _A_setup():
        B, S, D = 1, 2048, 512
        return {
            "x": _w((lambda *s: torch.randn(*s, dtype=torch.bfloat16) * 0.05)(B, S, D)),
            "wqkv": _w((lambda *s: torch.randn(*s, dtype=torch.bfloat16) * 0.05)(D, 3 * D)),
            "wo": _w((lambda *s: torch.randn(*s, dtype=torch.bfloat16) * 0.05)(D, D)),
        }
    _A_in = _A_setup()

    def _B_setup():
        B, S, D, F = 1, 2048, 512, 1376
        return {
            "x": _w((lambda *s: torch.randn(*s, dtype=torch.bfloat16) * 0.05)(B, S, D)),
            "w_gate": _w((lambda *s: torch.randn(*s, dtype=torch.bfloat16) * 0.05)(D, F)),
            "w_up": _w((lambda *s: torch.randn(*s, dtype=torch.bfloat16) * 0.05)(D, F)),
            "w_down": _w((lambda *s: torch.randn(*s, dtype=torch.bfloat16) * 0.05)(F, D)),
        }
    _B_in = _B_setup()

    return {
        "A_x": _A_in["x"],
        "A_wqkv": _A_in["wqkv"],
        "A_wo": _A_in["wo"],
        "B_x": _B_in["x"],
        "B_w_gate": _B_in["w_gate"],
        "B_w_up": _B_in["w_up"],
        "B_w_down": _B_in["w_down"],
    }

def reference(A_x, A_wqkv, A_wo, B_x, B_w_gate, B_w_up, B_w_down):
    def _A_ref(x, wqkv, wo):
        B, S, D = x.shape
        H = 32; Dh = D // H
        W, dil = 8, 2
        qkv = (lambda x, *d: x.permute(*d))((x @ wqkv).reshape(B, S, 3, H, Dh), 2, 0, 3, 1, 4)
        q, k, v = qkv[0], qkv[1], qkv[2]
        scale = Dh ** -0.5
        pos = torch.arange(S)
        off = torch.arange(W) * dil
        idx = pos[:, None] - off[None, :]
        mask = idx >= 0
        idx_c = torch.where(mask, idx, 0).reshape(-1)
        kg = k[:, :, idx_c, :].reshape(B, H, S, W, Dh)
        vg = v[:, :, idx_c, :].reshape(B, H, S, W, Dh)
        logits = torch.einsum("bhsd,bhswd->bhsw", q, kg) * scale
        logits = torch.where(mask[None, None, :, :], logits, -1e9)
        attn = _softmax(logits, axis=-1)
        o = torch.einsum("bhsw,bhswd->bhsd", attn, vg)
        return (lambda x, *d: x.permute(*d))(o, 0, 2, 1, 3).reshape(B, S, D) @ wo

    def _B_ref(x, w_gate, w_up, w_down):
        return (_gelu(x @ w_gate) * (x @ w_up)) @ w_down

    x = A_x
    x = x + _A_ref(_layernorm(x, axis=-1), A_wqkv, A_wo)
    x = x + _B_ref(_layernorm(x, axis=-1), B_w_gate, B_w_up, B_w_down)
    x = x + _A_ref(_layernorm(x, axis=-1), A_wqkv, A_wo)
    x = x + _B_ref(_layernorm(x, axis=-1), B_w_gate, B_w_up, B_w_down)
    return x
```

```cpp
#include <hip/hip_runtime.h>
#include <hip/hip_bf16.h>
typedef unsigned short u16;
__device__ __forceinline__ float bf2f(u16 b){ return __uint_as_float(((unsigned)b)<<16); }
__device__ __forceinline__ u16 f2bf(float f){ unsigned u=__float_as_uint(f); u += 0x7fffu + ((u>>16)&1u); return (u16)(u>>16); }
__device__ __forceinline__ float rbf(float f){ return bf2f(f2bf(f)); }

__global__ void ln_k(const u16* x, u16* y){
  int row = blockIdx.x; int l = threadIdx.x;
  float v[8]; float s=0;
  for(int j=0;j<8;j++){ v[j]=bf2f(x[row*512+l*8+j]); s+=v[j]; }
  for(int o=32;o>0;o>>=1) s+=__shfl_xor(s,o);
  float m=s/512.f; float q=0;
  for(int j=0;j<8;j++){ float d=v[j]-m; q+=d*d; }
  for(int o=32;o>0;o>>=1) q+=__shfl_xor(q,o);
  float var=rbf(q/512.f); float mb=rbf(m);
  float sd=rbf(sqrtf(rbf(var+1e-5f)));
  for(int j=0;j<8;j++) y[row*512+l*8+j]=f2bf(rbf(v[j]-mb)/sd);
}
__global__ void gemm_k(const u16* A,const u16* B,u16* C,const u16* R,int M,int N,int K){
  __shared__ float As[16][17], Bs[16][17];
  int tx=threadIdx.x, ty=threadIdx.y; int r=blockIdx.y*16+ty, c=blockIdx.x*16+tx; float acc=0;
  for(int k0=0;k0<K;k0+=16){
    As[ty][tx]=bf2f(A[r*K+k0+tx]); Bs[ty][tx]=bf2f(B[(k0+ty)*N+c]); __syncthreads();
    for(int k=0;k<16;k++) acc+=As[ty][k]*Bs[k][tx]; __syncthreads();
  }
  float o=rbf(acc); if(R) o=rbf(o+bf2f(R[r*N+c])); C[r*N+c]=f2bf(o);
}
__global__ void attn_k(const u16* qkv,u16* o){
  int id=blockIdx.x*blockDim.x+threadIdx.x; int s=id/32,h=id%32;
  float q[16]; for(int d=0;d<16;d++) q[d]=bf2f(qkv[s*1536+h*16+d]);
  float lg[8]; float mx=-1e30f;
  for(int w=0;w<8;w++){ int t=s-2*w; float a=0; if(t>=0){ for(int d=0;d<16;d++) a+=q[d]*bf2f(qkv[t*1536+512+h*16+d]); a=rbf(rbf(a)*0.25f);} else a=rbf(-1e9f); lg[w]=a; mx=fmaxf(mx,a);}
  float sum=0; for(int w=0;w<8;w++){ lg[w]=expf(lg[w]-mx); sum+=lg[w]; }
  for(int w=0;w<8;w++) lg[w]=rbf(lg[w]/sum);
  for(int d=0;d<16;d++){ float a=0; for(int w=0;w<8;w++){ int t=s-2*w; if(t<0)t=0; a+=lg[w]*bf2f(qkv[t*1536+1024+h*16+d]); } o[s*512+h*16+d]=f2bf(a);}
}
__global__ void geglu_k(const u16* g,const u16* u,u16* h,int n){
  int i=blockIdx.x*blockDim.x+threadIdx.x; if(i>=n) return; float x=bf2f(g[i]);
  float t=tanhf(0.7978845608028654f*(x+0.044715f*x*x*x)); float ge=rbf(0.5f*x*(1.f+t)); h[i]=f2bf(ge*bf2f(u[i]));
}
extern "C" void kernel_launch(void* const* d_in, const int* in_sizes, int n_in, void* d_out, int out_size, void* d_ws, size_t ws_size, hipStream_t stream) {
  const u16* x=(const u16*)d_in[0]; const u16* wqkv=(const u16*)d_in[1]; const u16* wo=(const u16*)d_in[2];
  const u16* wg=(const u16*)d_in[4]; const u16* wu=(const u16*)d_in[5]; const u16* wd=(const u16*)d_in[6];
  u16* ws=(u16*)d_ws; const int S=2048,D=512,F=1376;
  u16* xn=ws; u16* qkv=xn+S*D; u16* o=qkv+S*3*D; u16* x1=o+S*D; u16* g=x1+S*D; u16* u=g+S*F; u16* h=u+S*F; u16* x2=h+S*F;
  const u16* cur=x;
  for(int p=0;p<2;p++){
    ln_k<<<S,64,0,stream>>>(cur,xn);
    gemm_k<<<dim3(3*D/16,S/16),dim3(16,16),0,stream>>>(xn,wqkv,qkv,nullptr,S,3*D,D);
    attn_k<<<S*32/256,256,0,stream>>>(qkv,o);
    gemm_k<<<dim3(D/16,S/16),dim3(16,16),0,stream>>>(o,wo,x1,cur,S,D,D);
    ln_k<<<S,64,0,stream>>>(x1,xn);
    gemm_k<<<dim3(F/16,S/16),dim3(16,16),0,stream>>>(xn,wg,g,nullptr,S,F,D);
    gemm_k<<<dim3(F/16,S/16),dim3(16,16),0,stream>>>(xn,wu,u,nullptr,S,F,D);
    geglu_k<<<(S*F+255)/256,256,0,stream>>>(g,u,h,S*F);
    u16* dst = p==0? x2 : (u16*)d_out;
    gemm_k<<<dim3(D/16,S/16),dim3(16,16),0,stream>>>(h,wd,dst,x1,S,D,F);
    cur=x2;
  }
}
```

```cpp
#include <hip/hip_runtime.h>
#include <hip/hip_bf16.h>

#define GAS __attribute__((address_space(1)))
#define LAS __attribute__((address_space(3)))
#define DI __device__ __forceinline__
typedef unsigned v4u __attribute__((ext_vector_type(4)));
typedef unsigned v2u __attribute__((ext_vector_type(2)));
typedef float f32x2 __attribute__((ext_vector_type(2)));
typedef float f32x4 __attribute__((ext_vector_type(4)));
typedef float f32x16 __attribute__((ext_vector_type(16)));
typedef short s16x4 __attribute__((ext_vector_type(4)));
typedef short s16x8 __attribute__((ext_vector_type(8)));
typedef __bf16 bf16x8 __attribute__((ext_vector_type(8)));
typedef __bf16 bf16x2 __attribute__((ext_vector_type(2)));
typedef unsigned long long u64;
typedef unsigned short u16;
#define RLX_AGENT __ATOMIC_RELAXED, __HIP_MEMORY_SCOPE_AGENT

namespace {
constexpr int AUX_SC1 = 16;
constexpr int FLAG_OFF = 0;
constexpr int EPOCH_OFF = 65536;
constexpr int X_OFF = 1 << 20, X_SZ = 2 << 20;
constexpr int O_OFF = X_OFF + 4 * X_SZ;
constexpr int ST_OFF = O_OFF + 2 * X_SZ, ST_SZ = 64 * 16 * 256;
constexpr int H_OFF = ST_OFF + 4 * ST_SZ, H_SZ = 64 * 86 * 1024;
constexpr int WS_BYTES = H_OFF + 2 * H_SZ;
constexpr int PROF_OFF = 32 << 20;
constexpr int NR = 132;
constexpr int EX_OFF = NR * 1024, PLANE = 144 * 16;
constexpr int CS1_OFF = EX_OFF + 8 * PLANE, CS3_OFF = CS1_OFF + 96 * 4;
constexpr int LN_OFF = CS3_OFF + 192 * 4;
constexpr int KP_OFF = 87 * 1024;
constexpr int SY_OFF = LN_OFF + 16 * 8;
constexpr int SY_LANDED = SY_OFF;
constexpr int SY_CONS = SY_OFF + 16;
constexpr int SY_CONS5 = SY_OFF + 32;
constexpr int SY_READY = SY_OFF + 48, SY_PUB = SY_OFF + 52, SY_LN = SY_OFF + 60;
constexpr int SY_KV = SY_OFF + 64;
constexpr int SY_XF = SY_OFF + 80;
constexpr int SY_CS = SY_OFF + 96;
constexpr int SY_T4 = SY_OFF + 104;
constexpr int SY_DUMMY = SY_OFF + 128;
constexpr int LDS_BYTES = SY_DUMMY + 256;
constexpr int P_W1 = 0, P_W2 = 96, P_W3 = 160, P_W4 = 354, P_END = 528;
constexpr int pos2(int a) { return P_W2 + 4 * a; }
constexpr int pos3(int s) { return P_W3 + 6 * s + (s >= 17 ? 2 : 0); }
constexpr int pos4(int a) { return P_W4 + 4 * a + (a >= 10 ? 2 : 0); }
constexpr int T3 = 22, T4 = 32;
constexpr int slot_of(int p) { return (p % NR) * 1024; }

struct Params { const u16* x; const u16* wqkv; const u16* wo; const u16* wg; const u16* wu; const u16* wd; u16* out; unsigned char* ws; };

DI unsigned rfl(unsigned v) { return __builtin_amdgcn_readfirstlane(v); }
DI u64 uni64(u64 e) { return ((u64)rfl((unsigned)(e >> 32)) << 32) | rfl((unsigned)e); }
DI float asf(unsigned u) { return __builtin_bit_cast(float, u); }
DI unsigned asu(float f) { return __builtin_bit_cast(unsigned, f); }
DI unsigned pk(float a, float b) { bf16x2 v = {(__bf16)a, (__bf16)b}; return __builtin_bit_cast(unsigned, v); }
DI float bflo(unsigned u) { return asf(u << 16); }
DI float bfhi(unsigned u) { return asf(u & 0xffff0000u); }
DI float rbf(float a) { return bflo(pk(a, a)); }
DI float dot2(unsigned a, unsigned b, float c) { return __builtin_amdgcn_fdot2_f32_bf16(__builtin_bit_cast(bf16x2, a), __builtin_bit_cast(bf16x2, b), c, false); }
DI float dot2z(unsigned a, unsigned b) { float r; asm("v_dot2_f32_bf16 %0, %1, %2, 0" : "=v"(r) : "v"(a), "v"(b)); return r; }
DI float max3(float a, float b, float c) { float r; asm("v_max3_f32 %0, %1, %2, %3" : "=v"(r) : "v"(a), "v"(b), "v"(c)); return r; }
DI f32x2 fma2(f32x2 a, f32x2 b, f32x2 c) { return __builtin_elementwise_fma(a, b, c); }
DI f32x2 sp(float a) { return f32x2{a, a}; }
DI unsigned pk(f32x2 v) { return pk(v.x, v.y); }
template <int I> DI f32x2 pr(const f32x16& v) { return f32x2{v[2 * I], v[2 * I + 1]}; }
DI float sum32(float x) { auto r = __builtin_amdgcn_permlane32_swap(asu(x), asu(x), false, false); return asf(r[0]) + asf(r[1]); }
DI float sum16(float x) { auto r = __builtin_amdgcn_permlane16_swap(asu(x), asu(x), false, false); return asf(r[0]) + asf(r[1]); }
DI unsigned ror8(unsigned x) { return (unsigned)__builtin_amdgcn_update_dpp(0, (int)x, 0x128, 0xf, 0xf, false); }
DI f32x16 mfma32(s16x8 a, v4u b, f32x16 c) { return __builtin_amdgcn_mfma_f32_32x32x16_bf16(__builtin_bit_cast(bf16x8, a), __builtin_bit_cast(bf16x8, b), c, 0, 0, 0); }
DI f32x4 mfma16(s16x8 a, v4u b, f32x4 c) { return __builtin_amdgcn_mfma_f32_16x16x32_bf16(__builtin_bit_cast(bf16x8, a), __builtin_bit_cast(bf16x8, b), c, 0, 0, 0); }
DI s16x4 trr(LAS char* p) { return __builtin_amdgcn_ds_read_tr16_b64_v4i16((LAS s16x4*)p); }
DI s16x8 cat(s16x4 a, s16x4 b) { return __builtin_shufflevector(a, b, 0, 1, 2, 3, 4, 5, 6, 7); }
#define CBAR() asm volatile("" ::: "memory")
#define SBAR() __builtin_amdgcn_sched_barrier(0)
#define DRAIN() asm volatile("s_waitcnt vmcnt(0)" ::: "memory")

#define STAMP(i) do {} while (0)
#define PROF_DUMP() do {} while (0)
constexpr int LDS_ALLOC = LDS_BYTES;

#define CDECL() do {} while (0)
#define CST(site, i) do {} while (0)
#define CDUMP(site) do {} while (0)
template <int I> struct IC { static constexpr int v = I; };
template <int B, int E, class F> DI void sfor(F&& f) { if constexpr (B < E) { f(IC<B>{}); sfor<B + 1, E>(f); } }
DI int opq(int x) { asm volatile("" : "+s"(x)); return x; }
DI LAS char* opqv(LAS char* x) { asm volatile("" : "+v"(x)); return x; }

template <int OFF> DI void spin_ge(unsigned vs, unsigned need) {
  unsigned tv, ts, cnt;
  asm volatile("s_mov_b32 %2, 0x40000\n.Lsp%=:\n\tds_read_b32 %0, %3 offset:%5\n\ts_waitcnt lgkmcnt(0)\n\tv_readfirstlane_b32 %1, %0\n\t"
               "s_cmp_ge_u32 %1, %4\n\ts_cbranch_scc1 .Lsd%=\n\ts_sub_u32 %2, %2, 1\n\ts_cbranch_scc0 .Lsp%=\n.Lsd%=:"
               : "=&v"(tv), "=&s"(ts), "=&s"(cnt) : "v"(vs), "s"(need), "i"(OFF - SY_OFF) : "memory", "scc");
}
template <int OFF> DI void spin_all_ge(unsigned vs4, unsigned need) {
  unsigned tv, cnt;
  asm volatile("s_mov_b32 %1, 0x40000\n.Lsp%=:\n\tds_read_b32 %0, %2 offset:%4\n\ts_waitcnt lgkmcnt(0)\n\tv_cmp_gt_u32 vcc, %3, %0\n\t"
               "s_cbranch_vccz .Lsd%=\n\ts_sub_u32 %1, %1, 1\n\ts_cbranch_scc0 .Lsp%=\n.Lsd%=:"
               : "=&v"(tv), "=&s"(cnt) : "v"(vs4), "s"(need), "i"(OFF - SY_OFF) : "memory", "scc", "vcc");
}
DI void wait_landed(unsigned vs, unsigned& have, unsigned need) {
  unsigned t0, t1, cnt;
  asm volatile("s_cmp_ge_u32 %0, %5\n\ts_cbranch_scc0 .Lslow%=\n.Lback%=:\n\t.subsection 1\n.Lslow%=:\n\ts_mov_b32 %3, 0x40000\n.Lsp%=:\n\t"
               "ds_read_b32 %1, %4 offset:%6\n\tds_read_b32 %2, %4 offset:%6+4\n\ts_waitcnt lgkmcnt(0)\n\tv_min_u32 %1, %1, %2\n\ts_nop 0\n\tv_readfirstlane_b32 %0, %1\n\t"
               "s_cmp_ge_u32 %0, %5\n\ts_cbranch_scc1 .Lback%=\n\ts_sub_u32 %3, %3, 1\n\ts_cbranch_scc0 .Lsp%=\n\ts_branch .Lback%=\n\t.subsection 0"
               : "+s"(have), "=&v"(t0), "=&v"(t1), "=&s"(cnt) : "v"(vs), "s"(need), "i"(SY_LANDED - SY_OFF) : "memory", "scc");
}
template <int OFF> DI void sig(unsigned vs, unsigned val) {
  asm volatile("s_mov_b64 exec, 1\n\tds_write_b32 %0, %1 offset:%2\n\ts_mov_b64 exec, -1" :: "v"(vs), "v"(val), "i"(OFF - SY_OFF) : "memory");
}
DI void sigq(unsigned vq, unsigned val) { asm volatile("ds_write_b32 %0, %1" :: "v"(vq), "v"(val) : "memory"); }

DI void publish(const Params& p, LAS char* L, int lane, u64 val, unsigned nth) {
  DRAIN();
  if (lane == 0) {
    const unsigned old = __hip_atomic_fetch_add((LAS unsigned*)(L + SY_PUB), 1u, __ATOMIC_RELAXED, __HIP_MEMORY_SCOPE_WORKGROUP);
    if (old == 4u * nth + 3u) __hip_atomic_store((GAS u64*)(p.ws + FLAG_OFF + blockIdx.x * 256), val, RLX_AGENT);
  }
  CBAR();
}

DI v4u ln_stats(const v4u (&st)[4]) {
  f32x2 s = {0.f, 0.f};
#pragma unroll
  for (int i = 0; i < 4; ++i) s += f32x2{asf(st[i].x), asf(st[i].z)};
  const f32x2 mean = f32x2{sum32(sum16(s.x)), sum32(sum16(s.y))} * sp(1.f / 512.f);
  f32x2 m2 = {0.f, 0.f};
#pragma unroll
  for (int i = 0; i < 4; ++i) { const f32x2 d = fma2(f32x2{asf(st[i].x), asf(st[i].z)}, sp(1.f / 32.f), -mean); m2 += fma2(d * sp(32.f), d, f32x2{asf(st[i].y), asf(st[i].w)}); }
  const float v0 = sum32(sum16(m2.x)) * (1.f / 512.f), v1 = sum32(sum16(m2.y)) * (1.f / 512.f);
  const float sd0 = rbf(__builtin_amdgcn_sqrtf(rbf(rbf(v0) + 1e-5f))), sd1 = rbf(__builtin_amdgcn_sqrtf(rbf(rbf(v1) + 1e-5f)));
  return v4u{asu(__builtin_amdgcn_rcpf(sd0)), asu(-rbf(mean.x)), asu(__builtin_amdgcn_rcpf(sd1)), asu(-rbf(mean.y))};
}
struct LnSteps {
  v2u st[8]; f32x2 a, b, c, d; float S, R, m16, rstd, nmean;
  static constexpr int N = 22;
  DI f32x2 f(int i) const { return f32x2{asf(st[i].x), asf(st[i].y)}; }
  template <int I> DI void step() {
    if constexpr (I == 0) { a = f(0) + f(1); b = f(2) + f(3); }
    else if constexpr (I == 1) { c = f(4) + f(5); d = f(6) + f(7); }
    else if constexpr (I == 2) { a += b; c += d; }
    else if constexpr (I == 3) { a += c; }
    else if constexpr (I == 4) { S = sum32(a.x); }
    else if constexpr (I == 5) { m16 = S * (1.f / 16.f); nmean = S * (-1.f / 512.f); }
    else if constexpr (I == 6) { const float e = asf(st[0].x) - m16; R = e * e; }
    else if constexpr (I < 14) { const float e = asf(st[I - 6].x) - m16; R = __builtin_fmaf(e, e, R); }
    else if constexpr (I == 14) { R = __builtin_fmaf(R, 1.f / 32.f, a.y); }
    else if constexpr (I == 15) { R = sum32(R); }
    else if constexpr (I == 16) { R = rbf(R * (1.f / 512.f)); }
    else if constexpr (I == 17) { R = rbf(R + 1e-5f); }
    else if constexpr (I == 18) { R = __builtin_amdgcn_sqrtf(R); }
    else if constexpr (I == 19) { R = rbf(R); }
    else if constexpr (I == 20) { rstd = __builtin_amdgcn_rcpf(R); }
    else if constexpr (I == 21) { nmean = rbf(nmean); }
    if constexpr (I < 4) asm volatile("" : "+v"(a), "+v"(b)); if constexpr (I == 1 || I == 2) asm volatile("" : "+v"(c), "+v"(d));
    if constexpr (I == 4) asm volatile("" : "+v"(S)); if constexpr (I == 5) asm volatile("" : "+v"(m16), "+v"(nmean));
    if constexpr (I >= 6 && I < 20) asm volatile("" : "+v"(R)); if constexpr (I == 20) asm volatile("" : "+v"(rstd)); if constexpr (I == 21) asm volatile("" : "+v"(nmean));
  }
};
DI v4u ldx(__amdgpu_buffer_rsrc_t rs, int voff, int soff) { return __builtin_amdgcn_raw_buffer_load_b128(rs, voff, soff, AUX_SC1); }
DI v2u ldx2(__amdgpu_buffer_rsrc_t rs, int voff, int soff) { return __builtin_amdgcn_raw_buffer_load_b64(rs, voff, soff, AUX_SC1); }

#define MAPB 0
#define E1 8
#define E3 8
#define PFD 4
#define XW2 16
#define XW 8
#define DEPTH 20
struct Loader {
  unsigned vsl  , vs5  , cons; int pub;
#define LOOK "ds_read_b32 %1, %4 offset:%7\n\ts_waitcnt lgkmcnt(0)\n\t" \
             "v_min_u32_dpp %1, %1, %1 row_shr:4 row_mask:0xf bank_mask:0xf\n\ts_nop 1\n\t" \
             "v_min_u32_dpp %1, %1, %1 row_shr:2 row_mask:0xf bank_mask:0xf\n\ts_nop 1\n\t" \
             "v_min_u32_dpp %1, %1, %1 row_shr:1 row_mask:0xf bank_mask:0xf\n\ts_nop 0\n\t" \
             "v_readlane_b32 %0, %1, 7\n\ts_cmp_ge_u32 %0, %5\n\ts_cbranch_scc1 .Lback%=\n\t"
#define RUNG(N) "s_waitcnt vmcnt(" #N ")\n\ts_sub_i32 %2, %8, 2 * " #N " + 12\n\ts_max_i32 %3, %3, %2\n\tv_mov_b32 %1, %3\n\tds_write_b32 %6, %1\n\t" LOOK
  DI void room(unsigned pos, unsigned end) {
    unsigned tv, cnt;
    asm volatile("s_cmp_ge_i32 %0, %5\n\ts_cbranch_scc0 .Lslow%=\n.Lback%=:\n\t.subsection 1\n.Lslow%=:\n\t" LOOK RUNG(12) RUNG(6)
                 "s_waitcnt vmcnt(0)\n\tv_mov_b32 %1, %8\n\ts_mov_b32 %3, %8\n\tds_write_b32 %6, %1\n\ts_mov_b32 %2, 0x40000\n.Lsp%=:\n\t" LOOK
                 "s_sleep 1\n\ts_sub_u32 %2, %2, 1\n\ts_cbranch_scc0 .Lsp%=\n\ts_branch .Lback%=\n\t.subsection 0"
                 : "+s"(cons), "=&v"(tv), "=&s"(cnt), "+s"(pub) : "v"(vs5), "s"(end - NR), "v"(vsl), "i"(SY_CONS - SY_OFF), "s"(pos) : "memory", "scc");
  }
  DI void done(unsigned end) {
    pub = max((int)end - 2 * DEPTH - 12, pub);
    asm volatile("s_waitcnt vmcnt(%2)\n\tds_write_b32 %0, %1" :: "v"(vsl), "v"(pub), "i"(DEPTH) : "memory");
  }
};
#define GL1(v, sb) "s_nop 0\n\tglobal_load_lds_dwordx4 " v ", " sb "\n\t"
#define GLN(v, sb) "s_add_u32 m0, m0, 0x400\n\t" GL1(v, sb)
DI void loader_main(const Params& p, unsigned lb, int lane, int j, int l, int wave) {
  Loader ld; ld.vsl = lane == 0 ? lb + SY_LANDED + 4 * l : lb + SY_DUMMY + 4 * lane; ld.vs5 = lb + SY_OFF + 4 * min(lane & 7, 4);
  ld.cons = 0; ld.pub = 0;
  auto fresh = [&]() { int x = lane; asm volatile("" : "+v"(x)); return x; };
  auto off2 = [&]() { const int x = fresh(); return (unsigned)((x >> 3) * 1024 + (((x & 7) ^ (((x >> 4) & 1) * 4)) * 16)); };
  auto off3 = [&](unsigned (&og)[3], unsigned (&ou)[3]) {
    const int x = fresh();
#pragma unroll
    for (int q = 0; q < 3; ++q) { const int e = 64 * q + x, r = (e * 43691) >> 19, c = e - 12 * r; og[q] = r * 2752 + c * 16; ou[q] = r * 2752 + (c ^ 2) * 16; }
  };
  const int jc = j >> 1;
  const int us = (j / 3) * 16 + (j % 3) * 6;
  constexpr int N3 = (32 - T3) / 2, N4 = (43 - T4 + 1) / 2;
  v4u t3[N3][6], t4[N4][4];
  sfor<0, 2>([&](auto PASS) {
    constexpr int pass = PASS.v;
    constexpr unsigned pb = pass * P_END;
    const char* w = (const char*)p.wqkv + 64 * j + l * (32 * 3072);
    const int x1 = fresh(); const unsigned o1 = (x1 >> 2) * 3072 + (x1 & 3) * 16;
#pragma nounroll
    for (int s = 2 * l; s < 32; s += 4) {
      ld.room(pb + 3 * s, pb + 3 * s + 6);
      asm volatile("s_mov_b32 m0, %0\n\t" GL1("%1", "%4") GLN("%2", "%4") GLN("%3", "%4") GLN("%1", "%5") GLN("%2", "%5") GLN("%3", "%5")
                   :: "s"(lb + s * 3072), "v"(o1), "v"(o1 + 1024), "v"(o1 + 2048), "s"(w), "s"(w + 16 * 3072) : "memory", "scc");
      ld.done(pb + 3 * s + 6); w += 64 * 3072;
    }
    STAMP(40 + 8 * pass);
    w = (const char*)p.wo + 128 * jc + l * (16 * 1024);
    auto w2 = [&](int a0, int a1) {
      const unsigned o2 = off2();
#pragma nounroll
      for (int a = a0; a < a1; a += 2) {
        ld.room(pb + P_W2 + 4 * a, pb + P_W2 + 4 * a + 4);
        asm volatile("s_mov_b32 m0, %0\n\t" GL1("%1", "%5") GLN("%2", "%5") GLN("%3", "%5") GLN("%4", "%5")
                     :: "s"(lb + (a >= 9 ? a - 9 : a + 24) * 4096), "v"(o2), "v"(o2 + 8192), "v"(o2 + 256 * 1024), "v"(o2 + 256 * 1024 + 8192), "s"(w) : "memory", "scc");
        ld.done(pb + P_W2 + 4 * a + 4); w += 32 * 1024;
      }
    };
    w2(l, 9);
    if constexpr (pass == 0) {
      const __amdgpu_buffer_rsrc_t rg = __builtin_amdgcn_make_buffer_rsrc((void*)p.wg, 0, 512 * 2752, 0x00020000), ru = __builtin_amdgcn_make_buffer_rsrc((void*)p.wu, 0, 512 * 2752, 0x00020000);
      const int so = 32 * us + (T3 + l) * (16 * 2752);
      unsigned og[3], ou[3]; off3(og, ou);
      DRAIN(); ld.pub = P_W2 + 4 * (10 - l); sigq(ld.vsl, ld.pub);
#pragma unroll
      for (int i = 0; i < N3; ++i)
#pragma unroll
        for (int q = 0; q < 3; ++q) {
          t3[i][q] = __builtin_amdgcn_raw_buffer_load_b128(rg, og[q], so + i * (32 * 2752), 0);
          t3[i][3 + q] = __builtin_amdgcn_raw_buffer_load_b128(ru, ou[q], so + i * (32 * 2752), 0);
        }
      SBAR();
    }
    w2(10 - l, 16);
    STAMP(41 + 8 * pass);
    w = (const char*)p.wg + 32 * us + l * (16 * 2752);
    const char* wu = (const char*)p.wu + 32 * us + l * (16 * 2752);
    unsigned og[3], ou[3]; off3(og, ou);
#pragma nounroll
    for (int s = l; s < T3; s += 2) {
      const unsigned pos = pb + P_W3 + 6 * s + (s >= 17 ? 2 : 0);
      ld.room(pos, pos + 6);
      asm volatile("s_mov_b32 m0, %0\n\t" GL1("%1", "%7") GLN("%2", "%7") GLN("%3", "%7") GLN("%4", "%8") GLN("%5", "%8") GLN("%6", "%8")
                   :: "s"(lb + (s >= 17 ? s * 6144 - 17 * 6144 : s * 6144 + 28 * 1024)), "v"(og[0]), "v"(og[1]), "v"(og[2]), "v"(ou[0]), "v"(ou[1]), "v"(ou[2]), "s"(w), "s"(wu) : "memory", "scc");
      ld.done(pos + 6); w += 32 * 2752; wu += 32 * 2752;
    }
    if constexpr (pass == 0) {
      const __amdgpu_buffer_rsrc_t rd = __builtin_amdgcn_make_buffer_rsrc((void*)p.wd, 0, 1376 * 1024, 0x00020000);
      const int so = 128 * jc + (T4 + l) * (16 * 1024);
      const unsigned o2 = off2();
#pragma unroll
      for (int i = 0; i < N4; ++i)
#pragma unroll
        for (int q = 0; q < 4; ++q) t4[i][q] = __builtin_amdgcn_raw_buffer_load_b128(rd, o2, so + (i * 32 + (q & 1) * 8 + (q >> 1) * 688) * 1024, 0);
      asm volatile("s_waitcnt vmcnt(%0)" :: "i"(4 * N4) : "memory");
    } else DRAIN();
    LAS char* L3 = (LAS char*)(size_t)(lb + fresh() * 16 + slot_of(pos3(T3)) + l * 6144);
    sfor<0, N3>([&](auto I) {
      const unsigned pos = pb + pos3(T3 + 2 * I.v) + 6 * l;
      ld.pub = pos; sigq(ld.vsl, pos);
      ld.room(pos, pos + 6);
#pragma unroll
      for (int q = 0; q < 6; ++q) *(LAS v4u*)(L3 + I.v * 12288 + q * 1024) = t3[I.v][q];
    });
    ld.pub = pb + P_W4 + 4 * l; sigq(ld.vsl, ld.pub);
    STAMP(42 + 8 * pass);
    w = (const char*)p.wd + 128 * jc + l * (16 * 1024);
    const unsigned o2 = off2();
#pragma nounroll
    for (int a = l; a < T4; a += 2) {
      const unsigned pos = pb + P_W4 + 4 * a + (a >= 10 ? 2 : 0);
      ld.room(pos, pos + 4);
      asm volatile("s_mov_b32 m0, %0\n\t" GL1("%1", "%5") GLN("%2", "%5") GLN("%3", "%5") GLN("%4", "%5")
                   :: "s"(lb + (a >= 10 ? a * 4096 - 10 * 4096 : a * 4096 + 90 * 1024)), "v"(o2), "v"(o2 + 8192), "v"(o2 + 688 * 1024), "v"(o2 + 688 * 1024 + 8192), "s"(w) : "memory", "scc");
      ld.done(pos + 4); w += 32 * 1024;
    }
    STAMP(60);
    DRAIN();
    LAS char* L4 = (LAS char*)(size_t)(lb + fresh() * 16 + slot_of(pos4(T4)) + l * 4096);
    sfor<0, N4>([&](auto I) {
      if (I.v < N4 - 1 || l == 0) {
        const unsigned pos = pb + pos4(T4 + 2 * I.v) + 4 * l;
        ld.pub = pos; sigq(ld.vsl, pos);
        ld.room(pos, pos + 4);
#pragma unroll
        for (int q = 0; q < 4; ++q) *(LAS v4u*)(L4 + I.v * 8192 + q * 1024) = t4[I.v][q];
      }
    });
    ld.pub = pb + P_END; sigq(ld.vsl, ld.pub);
    STAMP(43 + 8 * pass);
  });
}

DI void helper_main(const Params& p, LAS char* L, unsigned vs, int lane, int j) {
  const int h = lane >> 5, gi = (lane >> 4) & 1, q = (lane & 15) >> 2, pp = lane & 3;
  const unsigned ones = 0x3f803f80u;
  unsigned have = 0;
  {
    LAS char* b1 = L + (4 * h + q) * 64 + gi * 32 + pp * 8;
    float a[6] = {0.f, 0.f, 0.f, 0.f, 0.f, 0.f};
#pragma nounroll
    for (int s = 0; s < 16; ++s) {
      wait_landed(vs, have, P_W1 + 6 * s + 6);
      v2u lo[6], hi[6];
#pragma unroll
      for (int c = 0; c < 6; ++c) { lo[c] = __builtin_bit_cast(v2u, trr(b1 + (6 * s + c) * 1024)); hi[c] = __builtin_bit_cast(v2u, trr(b1 + (6 * s + c) * 1024 + 512)); }
      SBAR();
#pragma unroll
      for (int c = 0; c < 6; ++c) a[c] = dot2(lo[c].x, ones, a[c]);
#pragma unroll
      for (int c = 0; c < 6; ++c) a[c] = dot2(lo[c].y, ones, a[c]);
#pragma unroll
      for (int c = 0; c < 6; ++c) a[c] = dot2(hi[c].x, ones, a[c]);
#pragma unroll
      for (int c = 0; c < 6; ++c) a[c] = dot2(hi[c].y, ones, a[c]);
    }
#pragma unroll
    for (int c = 0; c < 3; ++c) { const float t = sum32(a[c] + a[3 + c]); if (lane < 32) *(LAS float*)(L + CS1_OFF + (32 * c + lane) * 4) = t; }
    sig<SY_CS>(vs, 1u); sig<SY_CONS5>(vs, P_W3);
  }
  {
    const int bE = (4 * h + q) * 192 + 8 * pp + gi * (3072 + 32), bO = (4 * h + q) * 192 + 8 * pp + gi * (3072 - 32);
    const unsigned vq = lane == 0 ? vs + (SY_CONS5 - SY_OFF) : vs + (SY_DUMMY - SY_OFF) + 4 * lane;
    float a[6] = {0.f, 0.f, 0.f, 0.f, 0.f, 0.f};
    unsigned slot = P_W3 % NR, pos = P_W3;
#pragma nounroll
    for (int s = 0; s < 32; ++s) {
      if (s == 17) { slot = 0; pos += 2; }
      wait_landed(vs, have, pos + 6);
      LAS char* b = L + slot * 1024;
      v2u lo[6], hi[6];
#pragma unroll
      for (int u = 0; u < 6; ++u) { LAS char* bb = b + ((u & 1) ? bO : bE) + 32 * u; lo[u] = __builtin_bit_cast(v2u, trr(bb)); hi[u] = __builtin_bit_cast(v2u, trr(bb + 1536)); }
      pos += 6; slot += 6;
      sigq(vq, pos);
#pragma unroll
      for (int c = 0; c < 6; ++c) a[c] = dot2(lo[c].x, ones, a[c]);
#pragma unroll
      for (int c = 0; c < 6; ++c) a[c] = dot2(lo[c].y, ones, a[c]);
#pragma unroll
      for (int c = 0; c < 6; ++c) a[c] = dot2(hi[c].x, ones, a[c]);
#pragma unroll
      for (int c = 0; c < 6; ++c) a[c] = dot2(hi[c].y, ones, a[c]);
    }
#pragma unroll
    for (int u = 0; u < 6; ++u) { const float t = sum32(a[u]); if (lane < 32) *(LAS float*)(L + CS3_OFF + (32 * u + lane) * 4) = t; }
    sig<SY_CS>(vs, 2u); sig<SY_CONS5>(vs, 1u << 30);
  }
}

DI void poller_main(const Params& p, LAS char* L, unsigned vs, int lane, int t, int wave) {
  const __amdgpu_buffer_rsrc_t rs = __builtin_amdgcn_make_buffer_rsrc((void*)p.ws, 0, WS_BYTES, 0x00020000);
  const u64 E = uni64(__hip_atomic_load((GAS u64*)(p.ws + EPOCH_OFF), RLX_AGENT));
  auto wg_of = [](int tt, int jj) { return ((tt & 1) * 16 + jj) * 8 + (tt >> 1); };
  const int own = wg_of(t, lane & 15), prev = wg_of(t > 0 ? t - 1 : 0, lane & 15);
#pragma nounroll
  for (unsigned ph = 1; ph <= 8; ++ph) {
    spin_ge<SY_PUB>(vs, 4u * ph);
    const bool halo = (ph == 1 || ph == 5);
    GAS u64* f = (GAS u64*)(p.ws + FLAG_OFF + ((halo && lane >= 16) ? prev : own) * 256);
    for (unsigned n = 0; n < (1u << 16); ++n) {
      u64 v = E + ph;
      if (lane < (halo ? 32 : 16)) v = __hip_atomic_load(f, RLX_AGENT);
      if (__all(v - (E + ph) <= (u64)(8u - ph))) break;
    }
    __builtin_amdgcn_fence(__ATOMIC_ACQUIRE, "wavefront");
    sig<SY_READY>(vs, ph);
    STAMP(ph);
    if (halo) {
      const int hb = t > 0 ? 4 * t - 1 : 0, G = lane >> 4;
      const int hvo = (G >> 1) * 1024 + (16 + (lane & 15) + 32 * (G & 1)) * 16, xb = X_OFF + (ph >> 1) * X_SZ + hb * 32768;
      v4u a[4], hx[16];
#pragma unroll
      for (int i = 0; i < 4; ++i) a[i] = ldx(rs, lane * 16 + i * 1024, ST_OFF + (ph >> 1) * ST_SZ + hb * 4096);
#pragma unroll
      for (int i = 0; i < 16; ++i) hx[i] = ldx(rs, hvo + (i & 1) * 2048, xb + (i >> 1) * 4096);
      SBAR();
      const v4u r = ln_stats(a);
      if (lane >= 8 && lane < 16) *(LAS v4u*)(L + LN_OFF + (2 * lane - 16) * 8) = r;
#pragma unroll
      for (int i = 0; i < 16; ++i) *(LAS v4u*)(L + EX_OFF + i * 1024 + lane * 16) = hx[i];
      sig<SY_LN>(vs, ph);
    }
  }
  if (blockIdx.x == 0) {
    for (unsigned n = 0; n < (1u << 16); ++n) {
      bool ok = true;
#pragma unroll
      for (int i = 0; i < 4; ++i) ok &= (__hip_atomic_load((GAS u64*)(p.ws + FLAG_OFF + (i * 64 + lane) * 256), RLX_AGENT) - (E + 1) <= 7ull);
      if (__all(ok)) break;
    }
    if (lane == 0) __hip_atomic_store((GAS u64*)(p.ws + EPOCH_OFF), E + 16, RLX_AGENT);
  }
}

DI void stx(v4u v, __amdgpu_buffer_rsrc_t rs, int voff) { __builtin_amdgcn_raw_buffer_store_b128(v, rs, voff, 0, AUX_SC1); }
DI void stx2(v2u v, __amdgpu_buffer_rsrc_t rs, int voff) { __builtin_amdgcn_raw_buffer_store_b64(v, rs, voff, 0, AUX_SC1); }
template <int S> DI v4u ldf(__amdgpu_buffer_rsrc_t rs, int voff, int base) { return ldx(rs, voff + (S & 3) * 1024, base + (S >> 2) * 4096); }
struct LB { LAS char* b[3]; DI explicit LB(LAS char* x) { b[0] = opqv(x); b[1] = opqv(x + 60 * 1024); b[2] = opqv(x + 120 * 1024); }
  template <int OFF> DI LAS char* at() const { return b[OFF / (60 * 1024)] + OFF % (60 * 1024); } };

template <int NKS, int PW, bool IS_DOWN>
DI void proj_phase(const Params& p, LAS char* L, unsigned vs, __amdgpu_buffer_rsrc_t rs, int lane, int wave, int t, int j, int pass, unsigned& have,
                   int a_off  , int r_off  , int x_off, int st_off, unsigned ph  , u64 E) {
  const int m2 = wave & 1, kh = wave >> 1, th = j & 1, jc = j >> 1, tb = 4 * t + 2 * th + m2;
  const int h = lane >> 5, gi = (lane >> 4) & 1, q = (lane & 15) >> 2, pp = lane & 3;
  const unsigned pb = pass * P_END, vsw = vs + 4 * wave;
  const unsigned vq = lane == 0 ? vsw + (SY_CONS - SY_OFF) : vs + (SY_DUMMY - SY_OFF) + 4 * lane;
  constexpr int KT = IS_DOWN ? 86 : 32;
  const int ro = opq(r_off + (tb * 32 + 4 * jc + 2 * kh) * 1024);
  const v4u r0 = ldf<0>(rs, lane * 16, ro), r1 = ldf<1>(rs, lane * 16, ro);
  f32x16 acc[2];
#pragma unroll
  for (int i = 0; i < 4; ++i) { acc[0][2 * i] = bflo(r0[i]); acc[0][2 * i + 1] = bfhi(r0[i]); acc[0][8 + 2 * i] = bflo(r1[i]); acc[0][9 + 2 * i] = bfhi(r1[i]); }
#pragma unroll
  for (int i = 0; i < 16; ++i) acc[1][i] = 0.f;
  asm volatile("" : "+v"(acc[0]));
  auto posk = [](int a) { return IS_DOWN ? pos4(a) : pos2(a); };
  const int o0 = ((4 * h + q) * 128 + ((2 * gi + (pp >> 1)) ^ (4 * ((q >> 1) & 1))) * 16 + 8 * (pp & 1)) ^ (64 * kh);
  const LB b0(L + o0 + kh * 2048), b1(L + (o0 ^ 64) + kh * 2048);
  auto rd = [&](auto N) -> s16x8 {
    constexpr int a = N.v >> 1, c = N.v & 1, sl = slot_of(IS_DOWN ? pos4(a) : pos2(a));
    const LB& b = c ? b1 : b0;
    return cat(trr(b.template at<sl>()), trr(b.template at<sl>() + 1024));
  };
  constexpr int NOP = 2 * NKS, PF = PFD;
  s16x8 wf[PF];
  wait_landed(vs, have, pb + posk(8));
  sfor<0, PF>([&](auto N) { wf[N.v] = rd(N); });
  const int ab = opq(a_off + (tb * KT + kh * NKS) * 1024);
  SBAR();
  spin_ge<SY_READY>(vs, ph);
  STAMP(16 + 4 * ph);
  CDECL(); if (IS_DOWN) CST(4, 0);
  v4u xf[NKS];
  sfor<0, XW>([&](auto S) { xf[S.v] = ldf<S.v>(rs, lane * 16, ab); });
  SBAR();
  sfor<0, NOP>([&](auto N) {
    constexpr int n = N.v;
    const s16x8 cur = wf[n % PF];
    if constexpr (n + PF < NOP) {
      constexpr int a2 = (n + PF) >> 1;
      if constexpr (IS_DOWN && n + PF == 20) sigq(vq, pb + posk(10));
      if constexpr (((n + PF) & 15) == 0) wait_landed(vs, have, pb + posk(a2 + 8 < NKS ? a2 + 8 : NKS));
      wf[n % PF] = rd(IC<n + PF>{});
    }
    acc[n & 1] = mfma32(cur, xf[n >> 1], acc[n & 1]);
    constexpr int k = n < 2 * (XW2 - XW) ? XW + n : ((n & 1) ? XW2 + (n >> 1) : NKS);
    if constexpr (k < NKS) xf[k] = ldf<k>(rs, lane * 16, ab);
    if constexpr (IS_DOWN) { if constexpr (n == 1) CST(4, 1); if constexpr (n == 15) CST(4, 2); if constexpr (n == 31) CST(4, 3); if constexpr (n == 47) CST(4, 4); if constexpr (n == 63) CST(4, 5); if constexpr (n == 75) CST(4, 6); if constexpr (n == 85) CST(4, 7); }
    SBAR();
  });
  asm volatile("" : "+v"(acc[0]), "+v"(acc[1]));
  sig<SY_CONS>(vsw, pb + posk(NKS));
  if (IS_DOWN) CDUMP(4);
  STAMP(17 + 4 * ph);
#pragma unroll
  for (int g = 0; g < 4; ++g) *(LAS f32x4*)(L + EX_OFF + wave * 4096 + g * 1024 + lane * 16) = f32x4{acc[1][4 * g], acc[1][4 * g + 1], acc[1][4 * g + 2], acc[1][4 * g + 3]};
  sig<SY_XF>(vsw, ph);
  spin_ge<SY_XF>(vs + 4 * (wave ^ 2), ph);
  unsigned o[8]; f32x2 xv[8];
#pragma unroll
  for (int g = 0; g < 4; ++g) {
    const f32x4 e = *(LAS f32x4*)(L + EX_OFF + (wave ^ 2) * 4096 + g * 1024 + lane * 16);
    xv[2 * g] = f32x2{acc[0][4 * g], acc[0][4 * g + 1]} + f32x2{e[0], e[1]};
    xv[2 * g + 1] = f32x2{acc[0][4 * g + 2], acc[0][4 * g + 3]} + f32x2{e[2], e[3]};
    o[2 * g] = pk(xv[2 * g]); o[2 * g + 1] = pk(xv[2 * g + 1]);
  }
  if (IS_DOWN && pass == 1) {
    GAS char* dst = (GAS char*)p.out + (size_t)(tb * 32 + (lane & 31)) * 1024 + (64 * jc + 32 * kh + 4 * h) * 2;
#pragma unroll
    for (int g = 0; g < 4; ++g) *(GAS v2u*)(dst + 16 * g) = v2u{o[2 * g], o[2 * g + 1]};
    STAMP(18 + 4 * ph);
    return;
  }
  const int xo = x_off + (tb * 32 + 4 * jc + 2 * kh) * 1024 + lane * 16;
  stx(v4u{o[0], o[1], o[2], o[3]}, rs, xo);
  stx(v4u{o[4], o[5], o[6], o[7]}, rs, xo + 1024);
  const f32x2 s2 = ((xv[0] + xv[1]) + (xv[2] + xv[3])) + ((xv[4] + xv[5]) + (xv[6] + xv[7]));
  const float sum = sum32(s2.x + s2.y);
  const f32x2 mi = sp(sum * (-1.f / 32.f));
  f32x2 q0 = {0.f, 0.f}, q1 = {0.f, 0.f};
#pragma unroll
  for (int i = 0; i < 4; ++i) { const f32x2 d0 = xv[2 * i] + mi, d1 = xv[2 * i + 1] + mi; q0 = fma2(d0, d0, q0); q1 = fma2(d1, d1, q1); }
  q0 += q1;
  const float m2s = sum32(q0.x + q0.y);
  if (lane < 32) stx2(v2u{asu(sum), asu(m2s)}, rs, lane * 8 + st_off + (tb * 16 + 2 * jc + kh) * 256);
  STAMP(18 + 4 * ph);
  publish(p, L, lane, E + ph + 1, ph);
  STAMP(19 + 4 * ph);
}

DI void compute_main(const Params& p, LAS char* L, unsigned vs, int lane, int wave, int t, int j) {
  u64 E;
  const __amdgpu_buffer_rsrc_t rs = __builtin_amdgcn_make_buffer_rsrc((void*)p.ws, 0, WS_BYTES, 0x00020000);
  unsigned have = 0;
  const unsigned vsw = vs + 4 * wave;
  STAMP(10);
  {
    const int c = lane >> 3, tk = lane & 7, odd = c & 1, blk = 4 * t + (j >> 2), sl = 8 * (j & 3) + tk;
    const GAS char* src = (const GAS char*)p.x + (size_t)(128 * t + 8 * j + tk) * 1024 + (16 * wave + c) * 16;
    v4u d[2];
    d[0] = *(const GAS v4u*)src; d[1] = *(const GAS v4u*)(src + 128);
    const u64 e_ = __hip_atomic_load((GAS u64*)(p.ws + EPOCH_OFF), RLX_AGENT);
    SBAR();
#pragma unroll
    for (int a = 0; a < 2; ++a) {
      const int cI = 16 * wave + 8 * a + c;
      const unsigned r0 = ror8(odd ? d[a].x : d[a].z), r1 = ror8(odd ? d[a].y : d[a].w);
      const v4u o = odd ? v4u{r0, r1, d[a].z, d[a].w} : v4u{d[a].x, d[a].y, r0, r1};
      stx(o, rs, (cI >> 1) * 1024 + (sl + 32 * odd) * 16 + X_OFF + blk * 32768);
      float xv[8], s = 0.f;
#pragma unroll
      for (int i = 0; i < 4; ++i) { xv[2 * i] = bflo(d[a][i]); xv[2 * i + 1] = bfhi(d[a][i]); s += xv[2 * i] + xv[2 * i + 1]; }
      s += asf(ror8(asu(s))); s = sum16(s);
      const float mi = s * (1.f / 32.f); float m2 = 0.f;
#pragma unroll
      for (int i = 0; i < 8; ++i) { const float e = xv[i] - mi; m2 += e * e; }
      m2 += asf(ror8(asu(m2))); m2 = sum16(m2);
      if ((c & 3) == 0) stx2(v2u{asu(s), asu(m2)}, rs, (cI >> 2) * 256 + sl * 8 + ST_OFF + blk * 4096);
    }
    E = uni64(e_);
    STAMP(11);
    publish(p, L, lane, E + 1, 0);
    STAMP(12);
  }
#pragma nounroll
  for (int pass = 0; pass < 2; ++pass) {
    const unsigned pb = pass * P_END, ph = 4 * pass + 1;
    const int xin = X_OFF + 2 * pass * X_SZ, stin = ST_OFF + 2 * pass * ST_SZ;
    const int tb = 4 * t + wave;
    {
      int ln = lane; asm volatile("" : "+v"(ln));
      const int h = ln >> 5, gi = (ln >> 4) & 1, q = (ln & 15) >> 2, pp = ln & 3, tok = ln & 31;
      CDECL();
      v4u xf[32], hx[16]; LnSteps lns;
      const int xb = opq(xin + tb * 32768), sb = opq(stin + tb * 4096);
      const int G = ln >> 4;
      const LB b1(L + (4 * h + q) * 64 + gi * 32 + pp * 8);
      f32x16 acc[3];
#pragma unroll
      for (int c = 0; c < 3; ++c)
#pragma unroll
        for (int i = 0; i < 16; ++i) acc[c][i] = 0.f;
      constexpr int PF = PFD;
      s16x8 wf[PF];
      auto rd = [&](auto N) -> s16x8 { return cat(trr(b1.at<N.v * 1024>()), trr(b1.at<N.v * 1024>() + 512)); };
      const LB bh(L + (4 * (G & 1) + q) * 64 + (wave >> 1) * 32 + pp * 8 + (G >> 1) * 3072 + (1 + (wave & 1)) * 1024);
      auto rdh = [&](auto N) -> s16x8 { return cat(trr(bh.at<N.v * 6144>()), trr(bh.at<N.v * 6144>() + 512)); };
      f32x4 hacc = {0.f, 0.f, 0.f, 0.f};
      wait_landed(vs, have, pb + 24);
      sfor<0, PF>([&](auto N) { wf[N.v] = rd(N); });
      SBAR();
      spin_ge<SY_READY>(vs, ph);
      STAMP(16 + 4 * ph);
      CST(5, 0);
      sfor<0, XW>([&](auto S) { xf[S.v] = ldf<S.v>(rs, ln * 16, xb); });
      SBAR();
      CST(5, 1);
      sfor<0, 112>([&](auto N) {
        constexpr int n = N.v;
        const s16x8 cur = wf[n % PF];
        if constexpr (n + PF < 96) { if constexpr ((n + PF) % 24 == 0) wait_landed(vs, have, pb + n + PF + 24); wf[n % PF] = rd(IC<n + PF>{}); }
        else if constexpr (n + PF < 112) wf[n % PF] = rdh(IC<n + PF - 96>{});
        if constexpr (n < 96) acc[n % 3] = mfma32(cur, xf[n / 3], acc[n % 3]); else hacc = mfma16(cur, hx[n - 96], hacc);
        {
          constexpr int st_ = n / 3, k = n % 3 == 0 ? XW + st_ + (st_ < E1 ? st_ : E1) : (n % 3 == 2 && st_ < E1 ? XW + 2 * st_ + 1 : 32);
          if constexpr (k < 32) xf[k] = ldf<k>(rs, ln * 16, xb);
        }
        if constexpr (n % 3 == 1 && n / 3 < 8) lns.st[n / 3] = ldx2(rs, h * 2048 + tok * 8 + (n / 3) * 256, sb);
        if constexpr (n >= 44 && n % 2 == 0 && (n - 44) / 2 < LnSteps::N) lns.template step<(n - 44) / 2>();
        if constexpr (n == 89) spin_ge<SY_LN>(vs, ph);
        if constexpr (n >= 92 && n < 108) hx[n - 92] = *(LAS v4u*)(L + EX_OFF + (n - 92) * 1024 + ln * 16);
        if constexpr (n == 2) CST(5, 2); if constexpr (n == 23) CST(5, 3); if constexpr (n == 47) CST(5, 4); if constexpr (n == 71) CST(5, 5); if constexpr (n == 95) CST(5, 6); if constexpr (n == 111) CST(5, 7);
        SBAR();
      });
      asm volatile("" : "+v"(acc[0]), "+v"(acc[1]), "+v"(acc[2]), "+v"(hacc));
      sig<SY_CONS>(vsw, pb + KP_OFF / 1024);
      STAMP(17 + 4 * ph);
      CST(1, 0);
      spin_ge<SY_CS>(vs, 1u);
      CST(1, 1);
      const f32x2 lnh = *(LAS f32x2*)(L + LN_OFF + (ln & 15) * 8);
      const f32x2 rstd = sp(lns.rstd), nmean = sp(lns.nmean);
      unsigned pk_[8]; f32x2 vv[8];
#pragma unroll
      for (int g = 0; g < 4; ++g) {
        const f32x4 ck = *(LAS f32x4*)(L + CS1_OFF + (32 + 8 * g + 4 * h) * 4), cv = *(LAS f32x4*)(L + CS1_OFF + (64 + 8 * g + 4 * h) * 4);
        pk_[2 * g] = pk(fma2(f32x2{ck[0], ck[1]}, nmean, f32x2{acc[1][4 * g], acc[1][4 * g + 1]}) * rstd);
        pk_[2 * g + 1] = pk(fma2(f32x2{ck[2], ck[3]}, nmean, f32x2{acc[1][4 * g + 2], acc[1][4 * g + 3]}) * rstd);
        vv[2 * g] = fma2(f32x2{cv[0], cv[1]}, nmean, f32x2{acc[2][4 * g], acc[2][4 * g + 1]}) * rstd;
        vv[2 * g + 1] = fma2(f32x2{cv[2], cv[3]}, nmean, f32x2{acc[2][4 * g + 2], acc[2][4 * g + 3]}) * rstd;
      }
      f32x2 hv0, hv1;
      {
        const f32x4 cs = *(LAS f32x4*)(L + CS1_OFF + (32 * (1 + (wave & 1)) + 16 * (wave >> 1) + 4 * G) * 4);
        const f32x2 hr = sp(t == 0 ? 0.f : lnh.x);
        hv0 = fma2(f32x2{cs[0], cs[1]}, sp(lnh.y), f32x2{hacc[0], hacc[1]}) * hr; hv1 = fma2(f32x2{cs[2], cs[3]}, sp(lnh.y), f32x2{hacc[2], hacc[3]}) * hr;
        if (t == 0) { hv0 = sp(0.f); hv1 = sp(0.f); }
      }
      {
        const int row = (16 + 32 * wave + tok) * 16;
        CST(1, 2);
        spin_all_ge<SY_CONS>(vs + 4 * (ln & 3), pb + KP_OFF / 1024);
        CST(1, 3);
#pragma unroll
        for (int g = 0; g < 4; ++g) *(LAS f32x4*)(L + EX_OFF + ((g >> 1) * 4 + 2 * h + (g & 1)) * PLANE + row) = f32x4{vv[2 * g].x, vv[2 * g].y, vv[2 * g + 1].x, vv[2 * g + 1].y};
        if (wave & 1) *(LAS f32x4*)(L + EX_OFF + ((wave >> 1) * 4 + 2 * (G & 1) + (G >> 1)) * PLANE + (ln & 15) * 16) = f32x4{hv0.x, hv0.y, hv1.x, hv1.y};
        *(LAS v4u*)(L + KP_OFF + h * PLANE + row) = v4u{pk_[0], pk_[1], pk_[2], pk_[3]};
        *(LAS v4u*)(L + KP_OFF + (2 + h) * PLANE + row) = v4u{pk_[4], pk_[5], pk_[6], pk_[7]};
        if (!(wave & 1)) *(LAS v2u*)(L + KP_OFF + ((wave >> 1) * 2 + (G & 1)) * PLANE + (ln & 15) * 16 + 8 * (G >> 1)) = v2u{pk(hv0), pk(hv1)};
        sig<SY_KV>(vsw, pass + 1);
      }
      unsigned qq[8];
      {
        unsigned pq[8];
#pragma unroll
        for (int g = 0; g < 4; ++g) {
          const f32x4 cq = *(LAS f32x4*)(L + CS1_OFF + (8 * g + 4 * h) * 4);
          pq[2 * g] = pk(fma2(f32x2{cq[0], cq[1]}, nmean, f32x2{acc[0][4 * g], acc[0][4 * g + 1]}));
          pq[2 * g + 1] = pk(fma2(f32x2{cq[2], cq[3]}, nmean, f32x2{acc[0][4 * g + 2], acc[0][4 * g + 3]}));
        }
#pragma unroll
        for (int i = 0; i < 4; ++i) { auto r = __builtin_amdgcn_permlane32_swap(pq[i], pq[4 + i], false, false); qq[i] = r[0]; qq[4 + i] = r[1]; }
      }
      CST(1, 4);
      spin_all_ge<SY_KV>(vs + 4 * (ln & 3), pass + 1);
      CST(1, 5);
      LAS char* kb = L + KP_OFF + h * 2 * PLANE + (16 + 32 * wave + tok - 14) * 16;
      LAS char* vb = L + EX_OFF + h * 4 * PLANE + (16 + 32 * wave + tok - 14) * 16;
      float lg[8]; v4u kk[16]; f32x4 vr[32];
#pragma unroll
      for (int w = 0; w < 8; ++w) { kk[2 * w] = *(LAS v4u*)(kb + (14 - 2 * w) * 16); kk[2 * w + 1] = *(LAS v4u*)(kb + PLANE + (14 - 2 * w) * 16); }
      SBAR();
      sfor<0, 4>([&](auto W) {
        constexpr int w = 2 * W.v;
#pragma unroll
        for (int g = 0; g < 8; ++g) vr[4 * w + g] = *(LAS f32x4*)(vb + (g & 3) * PLANE + (14 - 2 * (w + (g >> 2))) * 16);
        float a = dot2z(qq[0], kk[2 * w].x), b = dot2z(qq[0], kk[2 * w + 2].x);
#pragma unroll
        for (int i = 1; i < 8; ++i) { a = dot2(qq[i], kk[2 * w + (i >> 2)][i & 3], a); b = dot2(qq[i], kk[2 * w + 2 + (i >> 2)][i & 3], b); }
        lg[w] = a; lg[w + 1] = b;
        SBAR();
      });
      asm volatile("" : "+v"(lg[0]), "+v"(lg[1]), "+v"(lg[2]), "+v"(lg[3]), "+v"(lg[4]), "+v"(lg[5]), "+v"(lg[6]), "+v"(lg[7]));
      sig<SY_CONS>(vsw, pb + P_W2);
      CST(1, 6);
      if (t == 0 && wave == 0) {
#pragma unroll
        for (int w = 1; w < 8; ++w) lg[w] = tok >= 2 * w ? lg[w] : -1e30f;
      }
      const float mx = max3(max3(lg[0], lg[1], lg[2]), max3(lg[3], lg[4], lg[5]), max3(lg[6], lg[7], lg[7]));
      const float sc = lns.rstd * (0.25f * 1.4426950408889634f);
      f32x2 e2[4];
#pragma unroll
      for (int w = 0; w < 4; ++w) { const f32x2 z = (f32x2{lg[2 * w], lg[2 * w + 1]} - sp(mx)) * sp(sc); e2[w] = f32x2{__builtin_amdgcn_exp2f(z.x), __builtin_amdgcn_exp2f(z.y)}; }
      const f32x2 s2 = (e2[0] + e2[1]) + (e2[2] + e2[3]);
      const f32x2 inv = sp(__builtin_amdgcn_rcpf(s2.x + s2.y));
      f32x2 ov[8];
#pragma unroll
      for (int w = 0; w < 8; ++w) {
        const f32x2 pw = sp(e2[w >> 1][w & 1]);
#pragma unroll
        for (int g = 0; g < 4; ++g) {
          const f32x4 v = vr[4 * w + g];
          ov[2 * g] = w ? fma2(f32x2{v[0], v[1]}, pw, ov[2 * g]) : f32x2{v[0], v[1]} * pw;
          ov[2 * g + 1] = w ? fma2(f32x2{v[2], v[3]}, pw, ov[2 * g + 1]) : f32x2{v[2], v[3]} * pw;
        }
      }
      const int oo = O_OFF + pass * X_SZ + (tb * 32 + 2 * j) * 1024 + h * 1024 + tok * 16;
      stx(v4u{pk(ov[0] * inv), pk(ov[1] * inv), pk(ov[2] * inv), pk(ov[3] * inv)}, rs, oo);
      stx(v4u{pk(ov[4] * inv), pk(ov[5] * inv), pk(ov[6] * inv), pk(ov[7] * inv)}, rs, oo + 512);
      CST(1, 7); CDUMP(1); CDUMP(5);
      STAMP(18 + 4 * ph);
      publish(p, L, lane, E + ph + 1, ph);
      STAMP(19 + 4 * ph);
    }
    { int ln = lane; asm volatile("" : "+v"(ln));
      proj_phase<16, P_W2, false>(p, L, vs, rs, ln, wave, t, j, pass, have, O_OFF + pass * X_SZ, xin, xin + X_SZ, stin + ST_SZ, ph + 1, E); }
    {
      int ln = lane; asm volatile("" : "+v"(ln));
      const int h = ln >> 5, gi = (ln >> 4) & 1, q = (ln & 15) >> 2, pp = ln & 3, tok = ln & 31;
      CDECL();
      v4u xf[32]; LnSteps lns;
      const int xb = opq(xin + X_SZ + tb * 32768), sb = opq(stin + ST_SZ + tb * 4096);
      const LB bE(L + (4 * h + q) * 192 + 8 * pp + gi * (3072 + 32)), bO(L + (4 * h + q) * 192 + 8 * pp + gi * (3072 - 32));
      f32x16 acc[6];
#pragma unroll
      for (int c = 0; c < 6; ++c)
#pragma unroll
        for (int i = 0; i < 16; ++i) acc[c][i] = 0.f;
      constexpr int PF = PFD;
      s16x8 wf[PF];
      auto rd = [&](auto N) -> s16x8 { constexpr int s = N.v / 6, u = N.v % 6; LAS char* b = ((u & 1) ? bO : bE).template at<slot_of(pos3(s))>() + 32 * u; return cat(trr(b), trr(b + 1536)); };
      const unsigned vq = ln == 0 ? vsw + (SY_CONS - SY_OFF) : vs + (SY_DUMMY - SY_OFF) + 4 * ln;
      wait_landed(vs, have, pb + pos3(4));
      sfor<0, PF>([&](auto N) { wf[N.v] = rd(N); });
      SBAR();
      spin_ge<SY_READY>(vs, ph + 2);
      STAMP(16 + 4 * (ph + 2));
      CST(3, 0);
      sfor<0, XW>([&](auto S) { xf[S.v] = ldf<S.v>(rs, ln * 16, xb); });
      SBAR();
      sfor<0, 192>([&](auto N) {
        constexpr int n = N.v;
        const s16x8 cur = wf[n % PF];
        if constexpr (n + PF < 192) {
          if constexpr ((n + PF) % 24 == 2 && n + PF > 24) sigq(vq, pb + pos3((n + PF) / 6));
          if constexpr ((n + PF) % 24 == 22 && n + PF < 24 * 7) wait_landed(vs, have, pb + pos3((n + PF) / 6 + 5));
          wf[n % PF] = rd(IC<n + PF>{});
        }
        acc[n % 6] = mfma32(cur, xf[n / 6], acc[n % 6]);
        {
          constexpr int st_ = n / 6, k = n % 6 == 0 ? XW + st_ + (st_ < E3 ? st_ : E3) : (n % 6 == 4 && st_ < E3 ? XW + 2 * st_ + 1 : 32);
          if constexpr (k < 32) xf[k] = ldf<k>(rs, ln * 16, xb);
        }
        if constexpr (n % 6 == 2 && n / 6 < 8) lns.st[n / 6] = ldx2(rs, h * 2048 + tok * 8 + (n / 6) * 256, sb);
        if constexpr (n >= 90 && n % 3 == 0 && (n - 90) / 3 < LnSteps::N) lns.template step<(n - 90) / 3>();
        if constexpr (n == 5) CST(3, 1); if constexpr (n == 47) CST(3, 2); if constexpr (n == 95) CST(3, 3); if constexpr (n == 119) CST(3, 4); if constexpr (n == 143) CST(3, 5); if constexpr (n == 167) CST(3, 6); if constexpr (n == 191) CST(3, 7);
        SBAR();
      });
      asm volatile("" : "+v"(acc[0]), "+v"(acc[1]), "+v"(acc[2]), "+v"(acc[3]), "+v"(acc[4]), "+v"(acc[5]));
      sig<SY_CONS>(vsw, pb + P_W4);
      CDUMP(3);
      STAMP(17 + 4 * (ph + 2));
      spin_ge<SY_CS>(vs, 2u);
      const f32x2 rstd = sp(lns.rstd), rstd2 = sp(lns.rstd * lns.rstd), nmean = sp(lns.nmean);
      const int us = (j / 3) * 16 + (j % 3) * 6, nu = (j % 3 == 2) ? 4 : 6;
      const int ho = H_OFF + pass * H_SZ + (tb * 86 + us) * 1024 + ln * 16;
#pragma unroll
      for (int u = 0; u < 6; ++u) {
        if (u < 4 || nu == 6) {
          unsigned hv[4];
#pragma unroll
          for (int g = 0; g < 2; ++g) {
            const f32x4 cg = *(LAS f32x4*)(L + CS3_OFF + (32 * u + 8 * g + 4 * h) * 4), cu = *(LAS f32x4*)(L + CS3_OFF + (32 * u + 16 + 8 * g + 4 * h) * 4);
#pragma unroll
            for (int i = 0; i < 2; ++i) {
              const f32x2 tg = fma2(f32x2{cg[2 * i], cg[2 * i + 1]}, nmean, f32x2{acc[u][4 * g + 2 * i], acc[u][4 * g + 2 * i + 1]});
              const f32x2 tu = fma2(f32x2{cu[2 * i], cu[2 * i + 1]}, nmean, f32x2{acc[u][8 + 4 * g + 2 * i], acc[u][8 + 4 * g + 2 * i + 1]});
              const f32x2 gv = tg * rstd;
              const f32x2 z = gv * fma2(gv * gv, sp(-0.1029432397f), sp(-2.3022081985f));
              const f32x2 d = f32x2{__builtin_amdgcn_exp2f(z.x), __builtin_amdgcn_exp2f(z.y)} + sp(1.f);
              hv[2 * g + i] = pk((tg * rstd2) * tu * f32x2{__builtin_amdgcn_rcpf(d.x), __builtin_amdgcn_rcpf(d.y)});
            }
          }
          stx(v4u{hv[0], hv[1], hv[2], hv[3]}, rs, ho + u * 1024);
        }
      }
      STAMP(18 + 4 * (ph + 2));
      publish(p, L, lane, E + ph + 3, ph + 2);
      STAMP(19 + 4 * (ph + 2));
    }
    { int ln = lane; asm volatile("" : "+v"(ln));
      proj_phase<43, P_W4, true>(p, L, vs, rs, ln, wave, t, j, pass, have, H_OFF + pass * H_SZ, xin + X_SZ, xin + 2 * X_SZ, stin + 2 * ST_SZ, ph + 3, E); }
  }
}

__global__ void __launch_bounds__(512, 2) fused_kernel(Params p) {
  extern __shared__ __attribute__((aligned(16))) char smem[];
  LAS char* L = (LAS char*)smem;
  const int lane = threadIdx.x & 63, wave = __builtin_amdgcn_readfirstlane(threadIdx.x >> 6);
  const int b = blockIdx.x;
  const int t = (b & 7) * 2 + (b >> 7), j = (b >> 3) & 15;
  asm volatile(".subsection 2\n\t.fill 128, 4, 0xBF800000\n\t.subsection 0");
  if (threadIdx.x < 32) *(LAS unsigned*)(L + SY_OFF + threadIdx.x * 4) = 0u;
  __syncthreads();
  const unsigned lb = (unsigned)(size_t)L;
  unsigned vs = lb + SY_OFF; asm volatile("" : "+v"(vs));
  if (wave < 4) { __builtin_amdgcn_s_setprio(2); compute_main(p, L, vs, lane, wave, t, j); }
  else if (wave == 4) { __builtin_amdgcn_s_setprio(3); poller_main(p, L, vs, lane, t, wave); }
  else if (wave == 5) helper_main(p, L, vs, lane, j);
  else loader_main(p, lb, lane, j, wave - 6, wave);
  PROF_DUMP();
}
}

extern "C" void kernel_launch(void* const* d_in, const int* in_sizes, int n_in, void* d_out, int out_size, void* d_ws, size_t ws_size, hipStream_t stream) {
  Params p;
  p.x = (const u16*)d_in[0]; p.wqkv = (const u16*)d_in[1]; p.wo = (const u16*)d_in[2];
  p.wg = (const u16*)d_in[4]; p.wu = (const u16*)d_in[5]; p.wd = (const u16*)d_in[6];
  p.out = (u16*)d_out; p.ws = (unsigned char*)d_ws;
  (void)hipFuncSetAttribute((const void*)fused_kernel, hipFuncAttributeMaxDynamicSharedMemorySize, LDS_ALLOC);
  hipLaunchKernelGGL(fused_kernel, dim3(256), dim3(512), LDS_ALLOC, stream, p);
}
```

```cpp
#include <hip/hip_runtime.h>
#include <hip/hip_bf16.h>

#define GAS __attribute__((address_space(1)))
#define LAS __attribute__((address_space(3)))
#define DI __device__ __forceinline__
typedef unsigned v4u __attribute__((ext_vector_type(4)));
typedef unsigned v2u __attribute__((ext_vector_type(2)));
typedef float f32x2 __attribute__((ext_vector_type(2)));
typedef float f32x4 __attribute__((ext_vector_type(4)));
typedef float f32x16 __attribute__((ext_vector_type(16)));
typedef short s16x4 __attribute__((ext_vector_type(4)));
typedef short s16x8 __attribute__((ext_vector_type(8)));
typedef __bf16 bf16x8 __attribute__((ext_vector_type(8)));
typedef __bf16 bf16x2 __attribute__((ext_vector_type(2)));
typedef unsigned long long u64;
typedef unsigned short u16;
#define RLX_AGENT __ATOMIC_RELAXED, __HIP_MEMORY_SCOPE_AGENT

namespace {
constexpr int AUX_SC1 = 16;
constexpr int FLAG_OFF = 0;
constexpr int EPOCH_OFF = 65536;
constexpr int X_OFF = 1 << 20, X_SZ = 2 << 20;
constexpr int O_OFF = X_OFF + 4 * X_SZ;
constexpr int ST_OFF = O_OFF + 2 * X_SZ, ST_SZ = 64 * 16 * 256;
constexpr int H_OFF = ST_OFF + 4 * ST_SZ, H_SZ = 64 * 86 * 1024;
constexpr int WS_BYTES = H_OFF + 2 * H_SZ;
constexpr int PROF_OFF = 32 << 20;
constexpr int NR = 132;
constexpr int EX_OFF = NR * 1024, PLANE = 144 * 16;
constexpr int CS1_OFF = EX_OFF + 8 * PLANE, CS3_OFF = CS1_OFF + 96 * 4;
constexpr int LN_OFF = CS3_OFF + 192 * 4;
constexpr int KP_OFF = 87 * 1024;
constexpr int SY_OFF = LN_OFF + 16 * 8;
constexpr int SY_LANDED = SY_OFF;
constexpr int SY_CONS = SY_OFF + 16;
constexpr int SY_HOLD = SY_OFF + 40, SY_READY = SY_OFF + 44;
constexpr int SY_PROG = SY_OFF + 48;
constexpr int SY_KV = SY_OFF + 64;
constexpr int SY_XF = SY_OFF + 80;
constexpr int SY_CS = SY_OFF + 96;
constexpr int SY_T4 = SY_OFF + 104;
constexpr int SY_LN = SY_OFF + 112;
constexpr int SY_DUMMY = SY_OFF + 128;
constexpr int LDS_BYTES = SY_DUMMY + 256;
constexpr int P_W1 = 0, P_W2 = 96, P_W3 = 160, P_W4 = 354, P_END = 528;
constexpr int pos2(int a) { return P_W2 + 4 * a; }
constexpr int pos3(int s) { return P_W3 + 6 * s + (s >= 17 ? 2 : 0); }
constexpr int pos4(int a) { return P_W4 + 4 * a + (a >= 10 ? 2 : 0); }
constexpr int T4 = 32;
constexpr int ustart(int j) { return (j / 3) * 16 + (j % 3) * 6; }
constexpr bool early_unit(int kh, int u) { for (int i = 0; i < 8; ++i) if (u >= ustart(8 * kh + i) && u < ustart(8 * kh + i) + 3) return true; return false; }
constexpr int ord4(int kh, int a) {
  if (a < 24) return ustart(8 * kh + a / 3) + a % 3;
  int n = 24;
  for (int u = 43 * kh; u < 43 * kh + 43; ++u) if (!early_unit(kh, u)) { if (n == a) return u; ++n; }
  return -1;
}
constexpr unsigned long long late8(int kh) { unsigned long long r = 0; for (int i = 0; i < 8; ++i) r |= (unsigned long long)ord4(kh, 24 + i) << (8 * i); return r; }
static_assert(ord4(0, 42) == 42 && ord4(1, 24) == 43 && ord4(1, 42) == 85, "unit order");
constexpr int slot_of(int p) { return (p % NR) * 1024; }

struct Params { const u16* x; const u16* wqkv; const u16* wo; const u16* wg; const u16* wu; const u16* wd; u16* out; unsigned char* ws; };

DI unsigned rfl(unsigned v) { return __builtin_amdgcn_readfirstlane(v); }
DI u64 uni64(u64 e) { return ((u64)rfl((unsigned)(e >> 32)) << 32) | rfl((unsigned)e); }
DI float asf(unsigned u) { return __builtin_bit_cast(float, u); }
DI unsigned asu(float f) { return __builtin_bit_cast(unsigned, f); }
DI unsigned pk(float a, float b) { bf16x2 v = {(__bf16)a, (__bf16)b}; return __builtin_bit_cast(unsigned, v); }
DI float bflo(unsigned u) { return asf(u << 16); }
DI float bfhi(unsigned u) { return asf(u & 0xffff0000u); }
DI float rbf(float a) { return bflo(pk(a, a)); }
DI float dot2(unsigned a, unsigned b, float c) { return __builtin_amdgcn_fdot2_f32_bf16(__builtin_bit_cast(bf16x2, a), __builtin_bit_cast(bf16x2, b), c, false); }
DI float dot2z(unsigned a, unsigned b) { float r; asm("v_dot2_f32_bf16 %0, %1, %2, 0" : "=v"(r) : "v"(a), "v"(b)); return r; }
DI float max3(float a, float b, float c) { float r; asm("v_max3_f32 %0, %1, %2, %3" : "=v"(r) : "v"(a), "v"(b), "v"(c)); return r; }
DI f32x2 fma2(f32x2 a, f32x2 b, f32x2 c) { return __builtin_elementwise_fma(a, b, c); }
DI f32x2 sp(float a) { return f32x2{a, a}; }
DI unsigned pk(f32x2 v) { return pk(v.x, v.y); }
template <int I> DI f32x2 pr(const f32x16& v) { return f32x2{v[2 * I], v[2 * I + 1]}; }
DI float sum32(float x) { auto r = __builtin_amdgcn_permlane32_swap(asu(x), asu(x), false, false); return asf(r[0]) + asf(r[1]); }
DI float sum16(float x) { auto r = __builtin_amdgcn_permlane16_swap(asu(x), asu(x), false, false); return asf(r[0]) + asf(r[1]); }
DI unsigned ror8(unsigned x) { return (unsigned)__builtin_amdgcn_update_dpp(0, (int)x, 0x128, 0xf, 0xf, false); }
DI f32x16 mfma32(s16x8 a, v4u b, f32x16 c) { return __builtin_amdgcn_mfma_f32_32x32x16_bf16(__builtin_bit_cast(bf16x8, a), __builtin_bit_cast(bf16x8, b), c, 0, 0, 0); }
DI f32x4 mfma16(s16x8 a, v4u b, f32x4 c) { return __builtin_amdgcn_mfma_f32_16x16x32_bf16(__builtin_bit_cast(bf16x8, a), __builtin_bit_cast(bf16x8, b), c, 0, 0, 0); }
DI s16x4 trr(LAS char* p) { return __builtin_amdgcn_ds_read_tr16_b64_v4i16((LAS s16x4*)p); }
DI s16x8 cat(s16x4 a, s16x4 b) { return __builtin_shufflevector(a, b, 0, 1, 2, 3, 4, 5, 6, 7); }
#define CBAR() asm volatile("" ::: "memory")
#define SBAR() __builtin_amdgcn_sched_barrier(0)
#define DRAIN() asm volatile("s_waitcnt vmcnt(0)" ::: "memory")

#define STAMP(i) do {} while (0)
#define PROF_DUMP() do {} while (0)
constexpr int LDS_ALLOC = LDS_BYTES;

#define CDECL() do {} while (0)
#define CST(site, i) do {} while (0)
#define CDUMP(site) do {} while (0)
template <int I> struct IC { static constexpr int v = I; };
template <int B, int E, class F> DI void sfor(F&& f) { if constexpr (B < E) { f(IC<B>{}); sfor<B + 1, E>(f); } }
DI int opq(int x) { asm volatile("" : "+s"(x)); return x; }
DI LAS char* opqv(LAS char* x) { asm volatile("" : "+v"(x)); return x; }

template <int OFF> DI void spin_ge(unsigned vs, unsigned need) {
  unsigned tv, ts, cnt;
  asm volatile("s_mov_b32 %2, 0x40000\n.Lsp%=:\n\tds_read_b32 %0, %3 offset:%5\n\ts_waitcnt lgkmcnt(0)\n\tv_readfirstlane_b32 %1, %0\n\t"
               "s_cmp_ge_u32 %1, %4\n\ts_cbranch_scc1 .Lsd%=\n\ts_sub_u32 %2, %2, 1\n\ts_cbranch_scc0 .Lsp%=\n.Lsd%=:"
               : "=&v"(tv), "=&s"(ts), "=&s"(cnt) : "v"(vs), "s"(need), "i"(OFF - SY_OFF) : "memory", "scc");
}
template <int OFF> DI void spin_all_ge(unsigned vs4, unsigned need) {
  unsigned tv, cnt;
  asm volatile("s_mov_b32 %1, 0x40000\n.Lsp%=:\n\tds_read_b32 %0, %2 offset:%4\n\ts_waitcnt lgkmcnt(0)\n\tv_cmp_gt_u32 vcc, %3, %0\n\t"
               "s_cbranch_vccz .Lsd%=\n\ts_sub_u32 %1, %1, 1\n\ts_cbranch_scc0 .Lsp%=\n.Lsd%=:"
               : "=&v"(tv), "=&s"(cnt) : "v"(vs4), "s"(need), "i"(OFF - SY_OFF) : "memory", "scc", "vcc");
}
DI void wait_landed(unsigned vs, unsigned& have, unsigned need) {
  unsigned t0, t1, cnt;
  asm volatile("s_cmp_ge_u32 %0, %5\n\ts_cbranch_scc0 .Lslow%=\n.Lback%=:\n\t.subsection 1\n.Lslow%=:\n\ts_mov_b32 %3, 0x40000\n.Lsp%=:\n\t"
               "ds_read_b32 %1, %4 offset:%6\n\tds_read_b32 %2, %4 offset:%6+4\n\ts_waitcnt lgkmcnt(0)\n\tv_min_u32 %1, %1, %2\n\ts_nop 0\n\tv_readfirstlane_b32 %0, %1\n\t"
               "s_cmp_ge_u32 %0, %5\n\ts_cbranch_scc1 .Lback%=\n\ts_sub_u32 %3, %3, 1\n\ts_cbranch_scc0 .Lsp%=\n\ts_branch .Lback%=\n\t.subsection 0"
               : "+s"(have), "=&v"(t0), "=&v"(t1), "=&s"(cnt) : "v"(vs), "s"(need), "i"(SY_LANDED - SY_OFF) : "memory", "scc");
}
template <int OFF> DI void sig(unsigned vs, unsigned val) {
  asm volatile("s_mov_b64 exec, 1\n\tds_write_b32 %0, %1 offset:%2\n\ts_mov_b64 exec, -1" :: "v"(vs), "v"(val), "i"(OFF - SY_OFF) : "memory");
}
DI void sigq(unsigned vq, unsigned val) { asm volatile("ds_write_b32 %0, %1" :: "v"(vq), "v"(val) : "memory"); }

DI unsigned peek(unsigned addr, int off) { return *(volatile LAS unsigned*)(size_t)(addr + off - SY_OFF); }
template <int OFF> DI void need_all(unsigned seen, unsigned vs4, unsigned need) {
  unsigned tv, cnt;
  asm volatile("v_cmp_gt_u32 vcc, %3, %5\n\ts_cbranch_vccnz .Lslow%=\n.Lback%=:\n\t.subsection 1\n.Lslow%=:\n\ts_mov_b32 %1, 0x40000\n.Lsp%=:\n\t"
               "ds_read_b32 %0, %2 offset:%4\n\ts_waitcnt lgkmcnt(0)\n\tv_cmp_gt_u32 vcc, %3, %0\n\t"
               "s_cbranch_vccz .Lback%=\n\ts_sub_u32 %1, %1, 1\n\ts_cbranch_scc0 .Lsp%=\n\ts_branch .Lback%=\n\t.subsection 0"
               : "=&v"(tv), "=&s"(cnt) : "v"(vs4), "s"(need), "i"(OFF - SY_OFF), "v"(seen) : "memory", "scc", "vcc");
}
DI v2u peek_landed(unsigned vs) { return *(volatile LAS v2u*)(size_t)(vs + SY_LANDED - SY_OFF); }
DI void take_landed(unsigned& have, v2u v) { asm volatile("s_max_u32 %0, %0, %1" : "+s"(have) : "s"(rfl(min(v.x, v.y))) : "scc"); }
DI void publish(unsigned vsw, unsigned ev) { DRAIN(); sig<SY_PROG>(vsw, ev); }

DI v4u ln_stats(const v4u (&st)[4]) {
  f32x2 s = {0.f, 0.f};
#pragma unroll
  for (int i = 0; i < 4; ++i) s += f32x2{asf(st[i].x), asf(st[i].z)};
  const f32x2 mean = f32x2{sum32(sum16(s.x)), sum32(sum16(s.y))} * sp(1.f / 512.f);
  f32x2 m2 = {0.f, 0.f};
#pragma unroll
  for (int i = 0; i < 4; ++i) { const f32x2 d = fma2(f32x2{asf(st[i].x), asf(st[i].z)}, sp(1.f / 32.f), -mean); m2 += fma2(d * sp(32.f), d, f32x2{asf(st[i].y), asf(st[i].w)}); }
  const float v0 = sum32(sum16(m2.x)) * (1.f / 512.f), v1 = sum32(sum16(m2.y)) * (1.f / 512.f);
  const float sd0 = rbf(__builtin_amdgcn_sqrtf(rbf(rbf(v0) + 1e-5f))), sd1 = rbf(__builtin_amdgcn_sqrtf(rbf(rbf(v1) + 1e-5f)));
  return v4u{asu(__builtin_amdgcn_rcpf(sd0)), asu(-rbf(mean.x)), asu(__builtin_amdgcn_rcpf(sd1)), asu(-rbf(mean.y))};
}
struct LnSteps {
  v2u st[8]; f32x2 a, b, c, d; float S, R, m16, rstd, nmean;
  static constexpr int N = 22;
  DI f32x2 f(int i) const { return f32x2{asf(st[i].x), asf(st[i].y)}; }
  template <int I> DI void step() {
    if constexpr (I == 0) { a = f(0) + f(1); b = f(2) + f(3); }
    else if constexpr (I == 1) { c = f(4) + f(5); d = f(6) + f(7); }
    else if constexpr (I == 2) { a += b; c += d; }
    else if constexpr (I == 3) { a += c; }
    else if constexpr (I == 4) { S = sum32(a.x); }
    else if constexpr (I == 5) { m16 = S * (1.f / 16.f); nmean = S * (-1.f / 512.f); }
    else if constexpr (I == 6) { const float e = asf(st[0].x) - m16; R = e * e; }
    else if constexpr (I < 14) { const float e = asf(st[I - 6].x) - m16; R = __builtin_fmaf(e, e, R); }
    else if constexpr (I == 14) { R = __builtin_fmaf(R, 1.f / 32.f, a.y); }
    else if constexpr (I == 15) { R = sum32(R); }
    else if constexpr (I == 16) { R = rbf(R * (1.f / 512.f)); }
    else if constexpr (I == 17) { R = rbf(R + 1e-5f); }
    else if constexpr (I == 18) { R = __builtin_amdgcn_sqrtf(R); }
    else if constexpr (I == 19) { R = rbf(R); }
    else if constexpr (I == 20) { rstd = __builtin_amdgcn_rcpf(R); }
    else if constexpr (I == 21) { nmean = rbf(nmean); }
    if constexpr (I < 4) asm volatile("" : "+v"(a), "+v"(b)); if constexpr (I == 1 || I == 2) asm volatile("" : "+v"(c), "+v"(d));
    if constexpr (I == 4) asm volatile("" : "+v"(S)); if constexpr (I == 5) asm volatile("" : "+v"(m16), "+v"(nmean));
    if constexpr (I >= 6 && I < 20) asm volatile("" : "+v"(R)); if constexpr (I == 20) asm volatile("" : "+v"(rstd)); if constexpr (I == 21) asm volatile("" : "+v"(nmean));
  }
};
DI v4u ldx(__amdgpu_buffer_rsrc_t rs, int voff, int soff) { return __builtin_amdgcn_raw_buffer_load_b128(rs, voff, soff, AUX_SC1); }
DI v2u ldx2(__amdgpu_buffer_rsrc_t rs, int voff, int soff) { return __builtin_amdgcn_raw_buffer_load_b64(rs, voff, soff, AUX_SC1); }

#define MAPB 0
#define AB 0
#define E1 8
#define E3 8
#define PFD 4
#define LATE0 28
#define XW 8
#define DEPTH 12
struct Loader {
  unsigned vsl  , vs6  , vs, cons; int pub;
#define LOOK "ds_read_b32 %1, %4 offset:%7\n\ts_waitcnt lgkmcnt(0)\n\t" \
             "v_min_u32_dpp %1, %1, %1 row_shr:4 row_mask:0xf bank_mask:0xf\n\ts_nop 1\n\t" \
             "v_min_u32_dpp %1, %1, %1 row_shr:2 row_mask:0xf bank_mask:0xf\n\ts_nop 1\n\t" \
             "v_min_u32_dpp %1, %1, %1 row_shr:1 row_mask:0xf bank_mask:0xf\n\ts_nop 0\n\t" \
             "v_readlane_b32 %0, %1, 7\n\ts_cmp_ge_u32 %0, %5\n\ts_cbranch_scc1 .Lback%=\n\t"
#define RUNG(N) "s_waitcnt vmcnt(" #N ")\n\ts_sub_i32 %2, %8, 2 * " #N " + 2\n\ts_max_i32 %3, %3, %2\n\tv_mov_b32 %1, %3\n\tds_write_b32 %6, %1\n\t" LOOK
  DI void room(unsigned pos, unsigned end) {
    unsigned tv, cnt;
    asm volatile("s_cmp_ge_i32 %0, %5\n\ts_cbranch_scc0 .Lslow%=\n.Lback%=:\n\t.subsection 1\n.Lslow%=:\n\t" LOOK RUNG(8) RUNG(4)
                 "s_waitcnt vmcnt(0)\n\tv_mov_b32 %1, %8\n\ts_mov_b32 %3, %8\n\tds_write_b32 %6, %1\n\ts_mov_b32 %2, 0x40000\n.Lsp%=:\n\t" LOOK
                 "s_sleep 1\n\ts_sub_u32 %2, %2, 1\n\ts_cbranch_scc0 .Lsp%=\n\ts_branch .Lback%=\n\t.subsection 0"
                 : "+s"(cons), "=&v"(tv), "=&s"(cnt), "+s"(pub) : "v"(vs6), "s"(end - NR), "v"(vsl), "i"(SY_CONS - SY_OFF), "s"(pos) : "memory", "scc");
  }
  unsigned hv, rv;
  DI void look() { asm volatile("ds_read_b32 %0, %2 offset:%3\n\tds_read_b32 %1, %2 offset:%4" : "=&v"(hv), "=&v"(rv) : "v"(vs), "i"(SY_HOLD - SY_OFF), "i"(SY_READY - SY_OFF) : "memory"); }
  DI void done(unsigned end) {
    pub = max((int)end - 2 * DEPTH - 2, pub);
    unsigned cnt;
    asm volatile("s_waitcnt vmcnt(%7) lgkmcnt(0)\n\tds_write_b32 %4, %5\n\tv_cmp_gt_u32 vcc, %0, %1\n\ts_cbranch_vccnz .Lslow%=\n.Lback%=:\n\t.subsection 1\n.Lslow%=:\n\t"
                 "s_waitcnt vmcnt(0)\n\tv_mov_b32 %0, %6\n\ts_mov_b32 %2, %6\n\tds_write_b32 %4, %0\n\ts_mov_b32 %3, 0x40000\n.Lsp%=:\n\t"
                 "ds_read_b32 %0, %8 offset:%9\n\tds_read_b32 %1, %8 offset:%10\n\ts_waitcnt lgkmcnt(0)\n\tv_cmp_gt_u32 vcc, %0, %1\n\ts_cbranch_vccz .Lback%=\n\t"
                 "s_sub_u32 %3, %3, 1\n\ts_cbranch_scc0 .Lsp%=\n\t"
                 "s_branch .Lback%=\n\t.subsection 0"
                 : "+v"(hv), "+v"(rv), "+s"(pub), "=&s"(cnt) : "v"(vsl), "v"(pub), "s"(end), "i"(DEPTH), "v"(vs), "i"(SY_HOLD - SY_OFF), "i"(SY_READY - SY_OFF) : "memory", "scc", "vcc");
  }
};
#define GL0(v, rs, so) "buffer_load_dwordx4 " v ", " rs ", " so " offen lds\n\t"
#define GL1(v, rs, so) "s_nop 0\n\t" GL0(v, rs, so)
#define GLN(v, rs, so) "s_add_u32 m0, m0, 0x800\n\t" GL1(v, rs, so)
DI v4u srd(const void* base, unsigned bytes) { const u64 a = (u64)base; return v4u{(unsigned)a, (unsigned)(a >> 32) & 0xFFFFu, bytes, 0x00020000u}; }
DI void loader_main(const Params& p, unsigned lb, int lane, int j, int l, int wave) {
  Loader ld; ld.vsl = lane == 0 ? lb + SY_LANDED + 4 * l : lb + SY_DUMMY + 4 * lane; ld.vs6 = lb + SY_OFF + 4 * min(lane & 7, 5);
  ld.cons = 0; ld.pub = 0; ld.vs = lb + SY_OFF;
  auto fresh = [&]() { int x = lane; asm volatile("" : "+v"(x)); return x; };
  auto off2 = [&]() { const int x = fresh(); return (unsigned)((x >> 3) * 1024 + (((x & 7) ^ (((x >> 4) & 1) * 4)) * 16)); };
  const int jc = j >> 1, us = (j / 3) * 16 + (j % 3) * 6;
  v4u w1[8][6];
  {
    const __amdgpu_buffer_rsrc_t rq = __builtin_amdgcn_make_buffer_rsrc((void*)p.wqkv, 0, 512 * 3072, 0x00020000);
    const int o1 = (lane >> 2) * 3072 + (lane & 3) * 16, so = 64 * j + l * (32 * 3072);
#pragma unroll
    for (int i = 0; i < 8; ++i)
#pragma unroll
      for (int q = 0; q < 6; ++q) w1[i][q] = __builtin_amdgcn_raw_buffer_load_b128(rq, o1 + (q % 3) * 1024, so + (i * 64 + (q / 3) * 16) * 3072, 0);
    SBAR();
  }
#pragma nounroll
  for (int pass = 0; pass < 2; ++pass) {
    const unsigned pb = pass * P_END;
    LAS char* L1 = (LAS char*)(size_t)(lb + fresh() * 16 + l * 6144);
    LAS char* L1b = opqv(L1 + 49152);
    sfor<0, 8>([&](auto I) {
      const unsigned pos = pb + 12 * I.v + 6 * l;
      ld.room(pos, pos + 6);
#pragma unroll
      for (int q = 0; q < 6; ++q) *(LAS v4u*)((I.v < 4 ? L1 : L1b) + (I.v & 3) * 12288 + q * 1024) = w1[I.v][q];
      ld.pub = I.v < 7 ? pos + 12 : pb + P_W2; sigq(ld.vsl, ld.pub);
    });
    STAMP(40 + 8 * pass);
    const unsigned lbl = lb + l * 1024;
    {
      const v4u r = srd(p.wo, 512 * 1024);
      const unsigned o2 = off2() + 128 * jc + l * 8192;
      unsigned m = lbl + 96 * 1024, pos = pb + P_W2, so = 0;
#pragma nounroll
      for (int a = 0; a < 16; a += 2) {
        ld.room(pos, pos + 8);
        ld.look();
        const unsigned m2 = a == 8 ? lbl : m + 4096;
        asm volatile("s_mov_b32 m0, %0\n\t" GL1("%2", "%6", "%7") GLN("%3", "%6", "%7") "s_mov_b32 m0, %1\n\t" GL1("%4", "%6", "%7") GLN("%5", "%6", "%7")
                     :: "s"(m), "s"(m2), "v"(o2), "v"(o2 + 256 * 1024), "v"(o2 + 16384), "v"(o2 + 256 * 1024 + 16384), "s"(r), "s"(so) : "memory", "scc");
        pos += 8; so += 32768; m = m2 + 4096;
        ld.done(pos);
      }
    }
    STAMP(41 + 8 * pass);
    {
      const v4u rg = srd(p.wg, 512 * 2752), ru = srd(p.wu, 512 * 2752), rm = l ? ru : rg;
      unsigned o3[3]; const int x = fresh();
#pragma unroll
      for (int q = 0; q < 3; ++q) {
        const int pi = 2 * q + l, up = pi >= 3, e = 64 * (pi - 3 * up) + x, r = (e * 43691) >> 19, c = e - 12 * r;
        o3[q] = r * 2752 + (c ^ (2 * up)) * 16 + 32 * us;
      }
      unsigned m = lbl + 28 * 1024, pos = pb + P_W3, so = 0;
#pragma nounroll
      for (int s = 0; s < 32; s += 2) {
        const unsigned end = pos + (s == 16 ? 14 : 12), m2 = s == 16 ? lbl : m + 6144;
        ld.room(pos, end);
        ld.look();
        asm volatile("s_mov_b32 m0, %0\n\t" GL1("%2", "%5", "%8") GLN("%3", "%6", "%8") GLN("%4", "%7", "%8") "s_mov_b32 m0, %1\n\t" GL1("%2", "%5", "%9") GLN("%3", "%6", "%9") GLN("%4", "%7", "%9")
                     :: "s"(m), "s"(m2), "v"(o3[0]), "v"(o3[1]), "v"(o3[2]), "s"(rg), "s"(rm), "s"(ru), "s"(so), "s"(so + 16 * 2752) : "memory", "scc");
        pos = end; so += 32 * 2752; m = m2 + 6144;
        ld.done(pos);
      }
    }
    STAMP(42 + 8 * pass);
    {
      const v4u r = srd(p.wd, 1376 * 1024);
      const unsigned o2 = off2() + 128 * jc + l * 8192;
      constexpr unsigned long long LT0 = late8(0), LT1 = late8(1);
      const int x = fresh(), xa = x & 31, kh = x >> 5, i3 = (xa * 43) >> 7, i = i3 + 8 * kh;
      const unsigned tab = (xa < 24 ? ((i * 43) >> 7) * -2 + 6 * i + xa - 3 * i3 : (int)((kh ? LT1 : LT0) >> (8 * (xa - 24))) & 255) * 16384;
      unsigned m = lbl + 90 * 1024, pos = pb + P_W4;
#pragma nounroll
      for (int a = 0; a < T4; a += 2) {
        if (a == 10) { m = lbl; pos += 2; }
        const unsigned s0 = __builtin_amdgcn_readlane(tab, a), s1 = __builtin_amdgcn_readlane(tab, a + 32), s2 = __builtin_amdgcn_readlane(tab, a + 1), s3 = __builtin_amdgcn_readlane(tab, a + 33);
        ld.room(pos, pos + 8);
        ld.look();
        asm volatile("s_mov_b32 m0, %0\n\ts_nop 3\n\t" GL0("%1", "%2", "%3") GLN("%1", "%2", "%4") GLN("%1", "%2", "%5") GLN("%1", "%2", "%6")
                     :: "s"(m), "v"(o2), "s"(r), "s"(s0), "s"(s1), "s"(s2), "s"(s3) : "memory", "scc");
        pos += 8; m += 8192;
        ld.done(pos);
      }
    }
    STAMP(60);
    DRAIN(); ld.pub = pb + P_END; sigq(ld.vsl, ld.pub);
    STAMP(43 + 8 * pass);
  }
}

struct Tail {
  v4u r[2 * (43 - T4)];
  DI void load(const Params& p, int lane, int j, int e) {
    const __amdgpu_buffer_rsrc_t rd = __builtin_amdgcn_make_buffer_rsrc((void*)p.wd, 0, 1376 * 1024, 0x00020000);
    const int o2 = (lane >> 3) * 1024 + (((lane & 7) ^ (((lane >> 4) & 1) * 4)) * 16);
    sfor<0, 2 * (43 - T4)>([&](auto I) {
      constexpr int u0 = ord4(0, T4 + (I.v >> 1)), u1 = ord4(1, T4 + (I.v >> 1));
      r[I.v] = __builtin_amdgcn_raw_buffer_load_b128(rd, o2, 128 * (j >> 1) + (e ? u1 : u0) * 16384 + (I.v & 1) * 8192, 0);
    });
    SBAR();
  }
  DI void put(LAS char* L, unsigned vs, int lane, int e, int pass) {
    spin_all_ge<SY_CONS>(vs + 4 * (lane & 3), pass * P_END + pos4(10));
#pragma unroll
    for (int i = 0; i < 2 * (43 - T4); ++i) *(LAS v4u*)(L + slot_of(pos4(T4)) + (4 * (i >> 1) + (i & 1)) * 1024 + e * 2048 + lane * 16) = r[i];
    sig<SY_T4>(vs + 4 * e, pass + 1);
  }
};
DI void colsum3(LAS char* L, unsigned vs, int lane, int e, unsigned& have) {
  const int h = lane >> 5, gi = (lane >> 4) & 1, q = (lane & 15) >> 2, pp = lane & 3;
  const unsigned ones = 0x3f803f80u;
  const int bE = (4 * h + q) * 192 + 8 * pp + gi * (3072 + 32), bO = (4 * h + q) * 192 + 8 * pp + gi * (3072 - 32);
  const int o0 = (e ? bO : bE) + 96 * e, o1 = (e ? bE : bO) + 96 * e + 32;
  const unsigned vq = lane == 0 ? vs + (SY_CONS + 16 - SY_OFF) + 4 * e : vs + (SY_DUMMY - SY_OFF) + 4 * lane;
  float a[6] = {0.f, 0.f, 0.f, 0.f, 0.f, 0.f};
  unsigned slot = P_W3 % NR, pos = P_W3;
#pragma nounroll
  for (int s = 0; s < 32; ++s) {
    if (s == 17) { slot = 0; pos += 2; }
    wait_landed(vs, have, pos + 6);
    LAS char* b = L + slot * 1024;
    v2u lo[3], hi[3];
#pragma unroll
    for (int u = 0; u < 3; ++u) { LAS char* bb = b + (u == 1 ? o1 : o0 + 32 * u); lo[u] = __builtin_bit_cast(v2u, trr(bb)); hi[u] = __builtin_bit_cast(v2u, trr(bb + 1536)); }
    pos += 6; slot += 6;
    sigq(vq, pos);
#pragma unroll
    for (int u = 0; u < 3; ++u) { a[u] = dot2(lo[u].x, ones, a[u]); a[3 + u] = dot2(hi[u].x, ones, a[3 + u]); }
#pragma unroll
    for (int u = 0; u < 3; ++u) { a[u] = dot2(lo[u].y, ones, a[u]); a[3 + u] = dot2(hi[u].y, ones, a[3 + u]); }
  }
#pragma unroll
  for (int u = 0; u < 3; ++u) { const float t = sum32(a[u] + a[3 + u]); if (lane < 32) *(LAS float*)(L + CS3_OFF + (32 * (3 * e + u) + lane) * 4) = t; }
  sig<SY_CS>(vs + 4 * e, 2u); sigq(vq, 1u << 30);
}

DI void helper_main(const Params& p, LAS char* L, unsigned vs, int lane, int j) {
  const int h = lane >> 5, gi = (lane >> 4) & 1, q = (lane & 15) >> 2, pp = lane & 3;
  const unsigned ones = 0x3f803f80u;
  unsigned have = 0;
  {
    LAS char* b1 = L + (4 * h + q) * 64 + gi * 32 + pp * 8;
    float a[6] = {0.f, 0.f, 0.f, 0.f, 0.f, 0.f};
#pragma nounroll
    for (int s = 0; s < 16; ++s) {
      wait_landed(vs, have, P_W1 + 6 * s + 6);
      v2u lo[6], hi[6];
#pragma unroll
      for (int c = 0; c < 6; ++c) { lo[c] = __builtin_bit_cast(v2u, trr(b1 + (6 * s + c) * 1024)); hi[c] = __builtin_bit_cast(v2u, trr(b1 + (6 * s + c) * 1024 + 512)); }
      SBAR();
#pragma unroll
      for (int c = 0; c < 6; ++c) a[c] = dot2(lo[c].x, ones, a[c]);
#pragma unroll
      for (int c = 0; c < 6; ++c) a[c] = dot2(lo[c].y, ones, a[c]);
#pragma unroll
      for (int c = 0; c < 6; ++c) a[c] = dot2(hi[c].x, ones, a[c]);
#pragma unroll
      for (int c = 0; c < 6; ++c) a[c] = dot2(hi[c].y, ones, a[c]);
    }
#pragma unroll
    for (int c = 0; c < 3; ++c) { const float t = sum32(a[c] + a[3 + c]); if (lane < 32) *(LAS float*)(L + CS1_OFF + (32 * c + lane) * 4) = t; }
    sig<SY_CS>(vs + 4, 1u); sig<SY_CONS + 20>(vs, P_W3);
  }
  Tail tl; tl.load(p, lane, j, 1);
  colsum3(L, vs, lane, 1, have);
  tl.put(L, vs, lane, 1, 0);
  tl.put(L, vs, lane, 1, 1);
}

DI void poller_main(const Params& p, LAS char* L, unsigned vs, int lane, int t, int j, int wave) {
  const __amdgpu_buffer_rsrc_t rs = __builtin_amdgcn_make_buffer_rsrc((void*)p.ws, 0, WS_BYTES, 0x00020000);
  sig<SY_CONS + 16>(vs, P_W3); sig<SY_CS>(vs, 1u);
  const u64 E = uni64(__hip_atomic_load((GAS u64*)(p.ws + EPOCH_OFF), RLX_AGENT));
  auto wg_of = [](int tt, int jj) { return ((tt & 1) * 16 + jj) * 8 + (tt >> 1); };
  const int own = wg_of(t, lane & 15), prev = wg_of(t > 0 ? t - 1 : 0, lane & 15);
  auto hop = [&](unsigned ev, bool halo) {
    spin_all_ge<SY_PROG>(vs + 4 * (lane & 3), ev);
    if (lane == 0) __hip_atomic_store((GAS u64*)(p.ws + FLAG_OFF + blockIdx.x * 256), E + ev, RLX_AGENT);
    GAS u64* f = (GAS u64*)(p.ws + FLAG_OFF + ((halo && lane >= 16) ? prev : own) * 256);
    for (unsigned n = 0; n < (1u << 16); ++n) {
      u64 v = E + ev;
      if (lane < (halo ? 32 : 16)) v = __hip_atomic_load(f, RLX_AGENT);
      if (__all(v - (E + ev) <= (u64)(10u - ev))) break;
    }
    __builtin_amdgcn_fence(__ATOMIC_ACQUIRE, "wavefront");
    sig<SY_READY>(vs, ev);
  };
  Tail tl; unsigned have = 0;
  sfor<0, 2>([&](auto PASS) {
    constexpr int pass = PASS.v;
    hop(1 + 5 * pass, true);
    STAMP(1 + 4 * pass);
    {
      const int hb = t > 0 ? 4 * t - 1 : 0, G = lane >> 4;
      const int hvo = (G >> 1) * 1024 + (16 + (lane & 15) + 32 * (G & 1)) * 16, xb = X_OFF + 2 * pass * X_SZ + hb * 32768;
      v4u a[4], hx[16];
#pragma unroll
      for (int i = 0; i < 4; ++i) a[i] = ldx(rs, lane * 16 + i * 1024, ST_OFF + 2 * pass * ST_SZ + hb * 4096);
#pragma unroll
      for (int i = 0; i < 16; ++i) hx[i] = ldx(rs, hvo + (i & 1) * 2048, xb + (i >> 1) * 4096);
      SBAR();
      const v4u r = ln_stats(a);
      if (lane >= 8 && lane < 16) *(LAS v4u*)(L + LN_OFF + (2 * lane - 16) * 8) = r;
#pragma unroll
      for (int i = 0; i < ((AB & 4) ? 1 : 16); ++i) *(LAS v4u*)(L + EX_OFF + i * 1024 + lane * 16) = hx[i];
      sig<SY_LN>(vs, pass + 1);
    }
    if constexpr (pass == 0) tl.load(p, lane, j, 0);
#pragma nounroll
    for (unsigned e = 2; e <= 5; ++e) {
      hop(e + 5 * pass, false);
      if (e != 5) STAMP(e + 4 * pass); else STAMP(62 + pass);
      if (pass == 0 && e == 3) colsum3(L, vs, lane, 0, have);
    }
    tl.put(L, vs, lane, 0, pass);
  });
  if (blockIdx.x == 0) {
    for (unsigned n = 0; n < (1u << 16); ++n) {
      bool ok = true;
#pragma unroll
      for (int i = 0; i < 4; ++i) ok &= (__hip_atomic_load((GAS u64*)(p.ws + FLAG_OFF + (i * 64 + lane) * 256), RLX_AGENT) - (E + 1) <= 9ull);
      if (__all(ok)) break;
    }
    if (lane == 0) __hip_atomic_store((GAS u64*)(p.ws + EPOCH_OFF), E + 16, RLX_AGENT);
  }
}

DI void stx(v4u v, __amdgpu_buffer_rsrc_t rs, int voff) { __builtin_amdgcn_raw_buffer_store_b128(v, rs, voff, 0, AUX_SC1); }
DI void stx2(v2u v, __amdgpu_buffer_rsrc_t rs, int voff) { __builtin_amdgcn_raw_buffer_store_b64(v, rs, voff, 0, AUX_SC1); }
template <int S> DI v4u ldf(__amdgpu_buffer_rsrc_t rs, int voff, int base) { return ldx(rs, voff + (S & 3) * 1024, base + (S >> 2) * 4096); }
struct LB { LAS char* b[3]; DI explicit LB(LAS char* x) { b[0] = opqv(x); b[1] = opqv(x + 60 * 1024); b[2] = opqv(x + 120 * 1024); }
  template <int OFF> DI LAS char* at() const { return b[OFF / (60 * 1024)] + OFF % (60 * 1024); } };

#define HOLDMASK 0x17
template <int PH> DI void hold(unsigned vs, unsigned ev) { if constexpr ((HOLDMASK >> PH) & 1) sig<SY_HOLD>(vs, ev); }
struct Resid { v4u a, b; };
DI Resid ld_resid(__amdgpu_buffer_rsrc_t rs, int lane, int wave, int t, int j, int r_off) {
  const int ro = opq(r_off + ((4 * t + 2 * (j & 1) + (wave & 1)) * 32 + 4 * (j >> 1) + 2 * (wave >> 1)) * 1024);
  return Resid{ldf<0>(rs, lane * 16, ro), ldf<1>(rs, lane * 16, ro)};
}
template <int NKS, bool IS_DOWN>
DI void proj_phase(const Params& p, LAS char* L, unsigned vs, __amdgpu_buffer_rsrc_t rs, int lane, int wave, int t, int j, int pass, unsigned& have,
                   int a_off  , const Resid& res, int x_off, int st_off, unsigned ev  , unsigned ph) {
  const int m2 = wave & 1, kh = wave >> 1, th = j & 1, jc = j >> 1, tb = 4 * t + 2 * th + m2;
  const int h = lane >> 5, gi = (lane >> 4) & 1, q = (lane & 15) >> 2, pp = lane & 3;
  const unsigned pb = pass * P_END, vsw = vs + 4 * wave;
  const unsigned vq = lane == 0 ? vsw + (SY_CONS - SY_OFF) : vs + (SY_DUMMY - SY_OFF) + 4 * lane;
  f32x16 acc[2];
#pragma unroll
  for (int i = 0; i < 4; ++i) { acc[0][2 * i] = bflo(res.a[i]); acc[0][2 * i + 1] = bfhi(res.a[i]); acc[0][8 + 2 * i] = bflo(res.b[i]); acc[0][9 + 2 * i] = bfhi(res.b[i]); }
#pragma unroll
  for (int i = 0; i < 16; ++i) acc[1][i] = 0.f;
  asm volatile("" : "+v"(acc[0]));
  auto posk = [](int a) { return IS_DOWN ? pos4(a) : pos2(a); };
  const int o0 = ((4 * h + q) * 128 + ((2 * gi + (pp >> 1)) ^ (4 * ((q >> 1) & 1))) * 16 + 8 * (pp & 1)) ^ (64 * kh);
  const LB b0(L + o0 + kh * 2048), b1(L + (o0 ^ 64) + kh * 2048);
  auto rd = [&](auto N) -> s16x8 {
    constexpr int a = N.v >> 1, c = N.v & 1, sl = slot_of(IS_DOWN ? pos4(a) : pos2(a));
    const LB& b = c ? b1 : b0;
    return cat(trr(b.template at<sl>()), trr(b.template at<sl>() + 1024));
  };
  constexpr int NOP = 2 * NKS, PF = PFD, LAST = IS_DOWN ? T4 : NKS;
  s16x8 wf[PF];
  wait_landed(vs, have, pb + posk(8));
  sfor<0, PF>([&](auto N) { wf[N.v] = rd(N); });
  const int ab = opq(a_off + tb * (IS_DOWN ? 86 : 32) * 1024 + (IS_DOWN ? 0 : kh * 16384)), khk = opq(kh * 1024);
  v4u xf[NKS];
  auto ldk = [&](auto K) {
    constexpr int u0 = ord4(0, K.v), u1 = ord4(1, K.v);
    if constexpr (IS_DOWN) xf[K.v] = ldx(rs, lane * 16, ab + u0 * 1024 + khk * (u1 - u0));
    else xf[K.v] = ldf<K.v>(rs, lane * 16, ab);
  };
  SBAR();
  spin_ge<SY_READY>(vs, ev);
  STAMP(16 + 4 * ph);
  CDECL(); if (IS_DOWN) CST(4, 0);
  sfor<0, XW>([&](auto S) { ldk(S); });
  SBAR();
  v2u pl; unsigned pf;
  sfor<0, NOP>([&](auto N) {
    constexpr int n = N.v;
    const s16x8 cur = wf[n % PF];
    if constexpr (n + PF < NOP) {
      constexpr int a2 = (n + PF) >> 1;
      if constexpr (IS_DOWN && n + PF == 20) sigq(vq, pb + posk(10));
      if constexpr (((n + PF) & 15) == 10 && a2 + 3 < LAST) pl = peek_landed(vs);
      if constexpr (((n + PF) & 15) == 15 && a2 + 1 < LAST) take_landed(have, pl);
      if constexpr (((n + PF) & 15) == 0 && a2 < LAST) wait_landed(vs, have, pb + posk(a2 + 8 < LAST ? a2 + 8 : LAST));
      if constexpr (IS_DOWN && n + PF == 2 * T4 - 5) pf = peek(vs + 4 * (lane & 1), SY_T4);
      if constexpr (IS_DOWN && n + PF == 2 * T4 - 1) need_all<SY_T4>(pf, vs + 4 * (lane & 1), pass + 1);
      wf[n % PF] = rd(IC<n + PF>{});
    }
    acc[n & 1] = mfma32(cur, xf[n >> 1], acc[n & 1]);
    if constexpr (IS_DOWN && n == LATE0 - 5) pf = peek(vs, SY_READY);
    if constexpr (IS_DOWN && n == LATE0 - 1) need_all<SY_READY>(pf, vs, ev + 1);
    constexpr int k = XW + n < 24 ? XW + n : (n >= LATE0 ? 24 + n - LATE0 : NKS);
    if constexpr (k < NKS) ldk(IC<k>{});
    if constexpr (IS_DOWN) { if constexpr (n == 1) CST(4, 1); if constexpr (n == 15) CST(4, 2); if constexpr (n == 31) CST(4, 3); if constexpr (n == 47) CST(4, 4); if constexpr (n == 63) CST(4, 5); if constexpr (n == 75) CST(4, 6); if constexpr (n == 85) CST(4, 7); }
    SBAR();
  });
  asm volatile("" : "+v"(acc[0]), "+v"(acc[1]));
  sigq(vq, pb + posk(NKS));
  const unsigned evo = ev + (IS_DOWN ? 2 : 1);
  if (!(IS_DOWN && pass == 1)) hold<IS_DOWN ? 4 : 2>(vs, evo);
  if (IS_DOWN) CDUMP(4);
  STAMP(17 + 4 * ph);
#pragma unroll
  for (int g = 0; g < 4; ++g) *(LAS f32x4*)(L + EX_OFF + wave * 4096 + g * 1024 + lane * 16) = f32x4{acc[1][4 * g], acc[1][4 * g + 1], acc[1][4 * g + 2], acc[1][4 * g + 3]};
  sig<SY_XF>(vsw, evo);
  spin_ge<SY_XF>(vs + 4 * (wave ^ 2), evo);
  unsigned o[8]; f32x2 xv[8];
#pragma unroll
  for (int g = 0; g < 4; ++g) {
    const f32x4 e = *(LAS f32x4*)(L + EX_OFF + (wave ^ 2) * 4096 + g * 1024 + lane * 16);
    xv[2 * g] = f32x2{acc[0][4 * g], acc[0][4 * g + 1]} + f32x2{e[0], e[1]};
    xv[2 * g + 1] = f32x2{acc[0][4 * g + 2], acc[0][4 * g + 3]} + f32x2{e[2], e[3]};
    o[2 * g] = pk(xv[2 * g]); o[2 * g + 1] = pk(xv[2 * g + 1]);
  }
  if (IS_DOWN && pass == 1) {
    GAS char* dst = (GAS char*)p.out + (size_t)(tb * 32 + (lane & 31)) * 1024 + (64 * jc + 32 * kh + 4 * h) * 2;
#pragma unroll
    for (int g = 0; g < 4; ++g) *(GAS v2u*)(dst + 16 * g) = v2u{o[2 * g], o[2 * g + 1]};
    STAMP(18 + 4 * ph);
    return;
  }
  const int xo = x_off + (tb * 32 + 4 * jc + 2 * kh) * 1024 + lane * 16;
  stx(v4u{o[0], o[1], o[2], o[3]}, rs, xo);
  stx(v4u{o[4], o[5], o[6], o[7]}, rs, xo + 1024);
  const f32x2 s2 = ((xv[0] + xv[1]) + (xv[2] + xv[3])) + ((xv[4] + xv[5]) + (xv[6] + xv[7]));
  const float sum = sum32(s2.x + s2.y);
  const f32x2 mi = sp(sum * (-1.f / 32.f));
  f32x2 q0 = {0.f, 0.f}, q1 = {0.f, 0.f};
#pragma unroll
  for (int i = 0; i < 4; ++i) { const f32x2 d0 = xv[2 * i] + mi, d1 = xv[2 * i + 1] + mi; q0 = fma2(d0, d0, q0); q1 = fma2(d1, d1, q1); }
  q0 += q1;
  const float m2s = sum32(q0.x + q0.y);
  if (lane < 32) stx2(v2u{asu(sum), asu(m2s)}, rs, lane * 8 + st_off + (tb * 16 + 2 * jc + kh) * 256);
  STAMP(18 + 4 * ph);
  publish(vsw, evo);
  STAMP(19 + 4 * ph);
}

DI void compute_main(const Params& p, LAS char* L, unsigned vs, int lane, int wave, int t, int j) {
  const __amdgpu_buffer_rsrc_t rs = __builtin_amdgcn_make_buffer_rsrc((void*)p.ws, 0, WS_BYTES, 0x00020000);
  unsigned have = 0;
  const unsigned vsw = vs + 4 * wave;
  STAMP(10);
  {
    const int c = lane >> 3, tk = lane & 7, odd = c & 1, blk = 4 * t + (j >> 2), sl = 8 * (j & 3) + tk;
    const GAS char* src = (const GAS char*)p.x + (size_t)(128 * t + 8 * j + tk) * 1024 + (16 * wave + c) * 16;
    v4u d[2];
    d[0] = *(const GAS v4u*)src; d[1] = *(const GAS v4u*)(src + 128);
    SBAR();
#pragma unroll
    for (int a = 0; a < 2; ++a) {
      const int cI = 16 * wave + 8 * a + c;
      const unsigned r0 = ror8(odd ? d[a].x : d[a].z), r1 = ror8(odd ? d[a].y : d[a].w);
      const v4u o = odd ? v4u{r0, r1, d[a].z, d[a].w} : v4u{d[a].x, d[a].y, r0, r1};
      stx(o, rs, (cI >> 1) * 1024 + (sl + 32 * odd) * 16 + X_OFF + blk * 32768);
      float xv[8], s = 0.f;
#pragma unroll
      for (int i = 0; i < 4; ++i) { xv[2 * i] = bflo(d[a][i]); xv[2 * i + 1] = bfhi(d[a][i]); s += xv[2 * i] + xv[2 * i + 1]; }
      s += asf(ror8(asu(s))); s = sum16(s);
      const float mi = s * (1.f / 32.f); float m2 = 0.f;
#pragma unroll
      for (int i = 0; i < 8; ++i) { const float e = xv[i] - mi; m2 += e * e; }
      m2 += asf(ror8(asu(m2))); m2 = sum16(m2);
      if ((c & 3) == 0) stx2(v2u{asu(s), asu(m2)}, rs, (cI >> 2) * 256 + sl * 8 + ST_OFF + blk * 4096);
    }
    hold<0>(vs, 1);
    STAMP(11);
    publish(vsw, 1);
    STAMP(12);
  }
#pragma nounroll
  for (int pass = 0; pass < 2; ++pass) {
    const unsigned pb = pass * P_END, ph = 4 * pass + 1, eb = 5 * pass + 1;
    Resid res;
    const int xin = X_OFF + 2 * pass * X_SZ, stin = ST_OFF + 2 * pass * ST_SZ;
    const int tb = 4 * t + wave;
    {
      int ln = lane; asm volatile("" : "+v"(ln));
      const int h = ln >> 5, gi = (ln >> 4) & 1, q = (ln & 15) >> 2, pp = ln & 3, tok = ln & 31;
      CDECL();
      v4u xf[32], hx[16]; LnSteps lns; if (AB & 2) { lns.rstd = 1.f; lns.nmean = 0.f; }
      const int xb = opq(xin + tb * 32768), sb = opq(stin + tb * 4096);
      const int G = ln >> 4;
      const LB b1(L + (4 * h + q) * 64 + gi * 32 + pp * 8);
      f32x16 acc[3];
#pragma unroll
      for (int c = 0; c < 3; ++c)
#pragma unroll
        for (int i = 0; i < 16; ++i) acc[c][i] = 0.f;
      constexpr int PF = PFD;
      s16x8 wf[PF];
      auto rd = [&](auto N) -> s16x8 { return cat(trr(b1.at<N.v * 1024>()), trr(b1.at<N.v * 1024>() + 512)); };
      const LB bh(L + (4 * (G & 1) + q) * 64 + (wave >> 1) * 32 + pp * 8 + (G >> 1) * 3072 + (1 + (wave & 1)) * 1024);
      auto rdh = [&](auto N) -> s16x8 { return cat(trr(bh.at<N.v * 6144>()), trr(bh.at<N.v * 6144>() + 512)); };
      f32x4 hacc = {0.f, 0.f, 0.f, 0.f};
      wait_landed(vs, have, pb + 24);
      sfor<0, PF>([&](auto N) { wf[N.v] = rd(N); });
      SBAR();
      spin_ge<SY_READY>(vs, eb);
      STAMP(16 + 4 * ph);
      CST(5, 0);
      v2u pl; unsigned pf, pc; f32x4 cs[12]; f32x4 csh; f32x2 lnh;
      sfor<0, XW>([&](auto S) { xf[S.v] = ldf<S.v>(rs, ln * 16, xb); });
      SBAR();
      CST(5, 1);
      sfor<0, 112>([&](auto N) {
        constexpr int n = N.v;
        const s16x8 cur = wf[n % PF];
        if constexpr (!(AB & 8) && (n + PF) % 24 == 17 && n + PF < 72) pl = peek_landed(vs);
        if constexpr (!(AB & 8) && (n + PF) % 24 == 23 && n + PF < 72) take_landed(have, pl);
        if constexpr (n + PF < 96) { if constexpr (!(AB & 8) && (n + PF) % 24 == 0) wait_landed(vs, have, pb + n + PF + 24); wf[n % PF] = rd(IC<n + PF>{}); }
        else if constexpr (n + PF < 112) wf[n % PF] = rdh(IC<n + PF - 96>{});
        if constexpr (n < 96) acc[n % 3] = mfma32(cur, xf[n / 3], acc[n % 3]); else hacc = mfma16(cur, hx[n - 96], hacc);
        {
          constexpr int st_ = n / 3, k = n % 3 == 0 ? XW + st_ + (st_ < E1 ? st_ : E1) : (n % 3 == 2 && st_ < E1 ? XW + 2 * st_ + 1 : 32);
          if constexpr (k < 32) { if constexpr (AB & 1) xf[k] = xf[k % 8]; else xf[k] = ldf<k>(rs, ln * 16, xb); }
        }
        if constexpr (!(AB & 2) && n % 3 == 1 && n / 3 < 8) lns.st[n / 3] = ldx2(rs, h * 2048 + tok * 8 + (n / 3) * 256, sb);
        if constexpr (!(AB & 2) && n >= 44 && n % 2 == 0 && (n - 44) / 2 < LnSteps::N) lns.template step<(n - 44) / 2>();
        if constexpr (n == 83) { pf = peek(vs, SY_LN); pc = peek(vs + 4, SY_CS); }
        if constexpr (n == 89) need_all<SY_LN>(pf, vs, pass + 1);
        if constexpr (n == 91) need_all<SY_CS>(pc, vs + 4, 1u);
        if constexpr (n >= 92 && n < 108) hx[n - 92] = *(LAS v4u*)(L + EX_OFF + (n - 92) * 1024 + ln * 16);
        if constexpr (n == 2) CST(5, 2); if constexpr (n == 23) CST(5, 3); if constexpr (n == 47) CST(5, 4); if constexpr (n == 71) CST(5, 5); if constexpr (n == 95) CST(5, 6); if constexpr (n == 111) CST(5, 7);
        SBAR();
      });
      asm volatile("" : "+v"(acc[0]), "+v"(acc[1]), "+v"(acc[2]), "+v"(hacc));
      sig<SY_CONS>(vsw, pb + KP_OFF / 1024);
      STAMP(17 + 4 * ph);
      CST(1, 0);
#pragma unroll
      for (int i = 0; i < 12; ++i) cs[i] = *(LAS f32x4*)(L + CS1_OFF + (8 * ((i + 4) % 12) + 4 * h) * 4);
      csh = *(LAS f32x4*)(L + CS1_OFF + (32 * (1 + (wave & 1)) + 16 * (wave >> 1) + 4 * G) * 4);
      lnh = *(LAS f32x2*)(L + LN_OFF + (ln & 15) * 8);
      res = ld_resid(rs, ln, wave, t, j, xin);
      SBAR();
      CST(1, 1);
      const f32x2 rstd = sp(lns.rstd), nmean = sp(lns.nmean);
      unsigned pk_[8]; f32x2 vv[8];
#pragma unroll
      for (int g = 0; g < 4; ++g) {
        const f32x4 ck = cs[g], cv = cs[4 + g];
        if (g == 2) pf = peek(vs + 4 * (ln & 3), SY_CONS);
        pk_[2 * g] = pk(fma2(f32x2{ck[0], ck[1]}, nmean, f32x2{acc[1][4 * g], acc[1][4 * g + 1]}) * rstd);
        pk_[2 * g + 1] = pk(fma2(f32x2{ck[2], ck[3]}, nmean, f32x2{acc[1][4 * g + 2], acc[1][4 * g + 3]}) * rstd);
        vv[2 * g] = fma2(f32x2{cv[0], cv[1]}, nmean, f32x2{acc[2][4 * g], acc[2][4 * g + 1]}) * rstd;
        vv[2 * g + 1] = fma2(f32x2{cv[2], cv[3]}, nmean, f32x2{acc[2][4 * g + 2], acc[2][4 * g + 3]}) * rstd;
      }
      f32x2 hv0, hv1;
      {
        const f32x4 cs = csh;
        const f32x2 hr = sp(t == 0 ? 0.f : lnh.x);
        hv0 = fma2(f32x2{cs[0], cs[1]}, sp(lnh.y), f32x2{hacc[0], hacc[1]}) * hr; hv1 = fma2(f32x2{cs[2], cs[3]}, sp(lnh.y), f32x2{hacc[2], hacc[3]}) * hr;
        if (t == 0) { hv0 = sp(0.f); hv1 = sp(0.f); }
      }
      {
        const int row = (16 + 32 * wave + tok) * 16;
        CST(1, 2);
        need_all<SY_CONS>(pf, vs + 4 * (ln & 3), pb + KP_OFF / 1024);
        CST(1, 3);
#pragma unroll
        for (int g = 0; g < 4; ++g) *(LAS f32x4*)(L + EX_OFF + ((g >> 1) * 4 + 2 * h + (g & 1)) * PLANE + row) = f32x4{vv[2 * g].x, vv[2 * g].y, vv[2 * g + 1].x, vv[2 * g + 1].y};
        if (wave & 1) *(LAS f32x4*)(L + EX_OFF + ((wave >> 1) * 4 + 2 * (G & 1) + (G >> 1)) * PLANE + (ln & 15) * 16) = f32x4{hv0.x, hv0.y, hv1.x, hv1.y};
        *(LAS v4u*)(L + KP_OFF + h * PLANE + row) = v4u{pk_[0], pk_[1], pk_[2], pk_[3]};
        *(LAS v4u*)(L + KP_OFF + (2 + h) * PLANE + row) = v4u{pk_[4], pk_[5], pk_[6], pk_[7]};
        if (!(wave & 1)) *(LAS v2u*)(L + KP_OFF + ((wave >> 1) * 2 + (G & 1)) * PLANE + (ln & 15) * 16 + 8 * (G >> 1)) = v2u{pk(hv0), pk(hv1)};
        sig<SY_KV>(vsw, pass + 1);
      }
      unsigned qq[8];
      {
        unsigned pq[8];
#pragma unroll
        for (int g = 0; g < 4; ++g) {
          const f32x4 cq = cs[8 + g];
          pq[2 * g] = pk(fma2(f32x2{cq[0], cq[1]}, nmean, f32x2{acc[0][4 * g], acc[0][4 * g + 1]}));
          pq[2 * g + 1] = pk(fma2(f32x2{cq[2], cq[3]}, nmean, f32x2{acc[0][4 * g + 2], acc[0][4 * g + 3]}));
        }
#pragma unroll
        for (int i = 0; i < 4; ++i) { auto r = __builtin_amdgcn_permlane32_swap(pq[i], pq[4 + i], false, false); qq[i] = r[0]; qq[4 + i] = r[1]; }
      }
      CST(1, 4);
      spin_all_ge<SY_KV>(vs + 4 * (ln & 3), pass + 1);
      CST(1, 5);
      LAS char* kb = L + KP_OFF + h * 2 * PLANE + (16 + 32 * wave + tok - 14) * 16;
      LAS char* vb = L + EX_OFF + h * 4 * PLANE + (16 + 32 * wave + tok - 14) * 16;
      float lg[8]; v4u kk[16]; f32x4 vr[32];
#pragma unroll
      for (int w = 0; w < 8; ++w) { kk[2 * w] = *(LAS v4u*)(kb + (14 - 2 * w) * 16); kk[2 * w + 1] = *(LAS v4u*)(kb + PLANE + (14 - 2 * w) * 16); }
      SBAR();
      sfor<0, 4>([&](auto W) {
        constexpr int w = 2 * W.v;
#pragma unroll
        for (int g = 0; g < 8; ++g) vr[4 * w + g] = *(LAS f32x4*)(vb + (g & 3) * PLANE + (14 - 2 * (w + (g >> 2))) * 16);
        float a = dot2z(qq[0], kk[2 * w].x), b = dot2z(qq[0], kk[2 * w + 2].x);
#pragma unroll
        for (int i = 1; i < 8; ++i) { a = dot2(qq[i], kk[2 * w + (i >> 2)][i & 3], a); b = dot2(qq[i], kk[2 * w + 2 + (i >> 2)][i & 3], b); }
        lg[w] = a; lg[w + 1] = b;
        SBAR();
      });
      asm volatile("" : "+v"(lg[0]), "+v"(lg[1]), "+v"(lg[2]), "+v"(lg[3]), "+v"(lg[4]), "+v"(lg[5]), "+v"(lg[6]), "+v"(lg[7]));
      sig<SY_CONS>(vsw, pb + P_W2);
      hold<1>(vs, eb + 1);
      CST(1, 6);
      if (t == 0 && wave == 0) {
#pragma unroll
        for (int w = 1; w < 8; ++w) lg[w] = tok >= 2 * w ? lg[w] : -1e30f;
      }
      const float mx = max3(max3(lg[0], lg[1], lg[2]), max3(lg[3], lg[4], lg[5]), max3(lg[6], lg[7], lg[7]));
      const float sc = lns.rstd * (0.25f * 1.4426950408889634f);
      f32x2 e2[4];
#pragma unroll
      for (int w = 0; w < 4; ++w) { const f32x2 z = (f32x2{lg[2 * w], lg[2 * w + 1]} - sp(mx)) * sp(sc); e2[w] = f32x2{__builtin_amdgcn_exp2f(z.x), __builtin_amdgcn_exp2f(z.y)}; }
      const f32x2 s2 = (e2[0] + e2[1]) + (e2[2] + e2[3]);
      const f32x2 inv = sp(__builtin_amdgcn_rcpf(s2.x + s2.y));
      f32x2 ov[8];
#pragma unroll
      for (int w = 0; w < 8; ++w) {
        const f32x2 pw = sp(e2[w >> 1][w & 1]);
#pragma unroll
        for (int g = 0; g < 4; ++g) {
          const f32x4 v = vr[4 * w + g];
          ov[2 * g] = w ? fma2(f32x2{v[0], v[1]}, pw, ov[2 * g]) : f32x2{v[0], v[1]} * pw;
          ov[2 * g + 1] = w ? fma2(f32x2{v[2], v[3]}, pw, ov[2 * g + 1]) : f32x2{v[2], v[3]} * pw;
        }
      }
      const int oo = O_OFF + pass * X_SZ + (tb * 32 + 2 * j) * 1024 + h * 1024 + tok * 16;
      stx(v4u{pk(ov[0] * inv), pk(ov[1] * inv), pk(ov[2] * inv), pk(ov[3] * inv)}, rs, oo);
      stx(v4u{pk(ov[4] * inv), pk(ov[5] * inv), pk(ov[6] * inv), pk(ov[7] * inv)}, rs, oo + 512);
      CST(1, 7); CDUMP(1); CDUMP(5);
      STAMP(18 + 4 * ph);
      publish(vsw, eb + 1);
      STAMP(19 + 4 * ph);
    }
    { int ln = lane; asm volatile("" : "+v"(ln));
      proj_phase<16, false>(p, L, vs, rs, ln, wave, t, j, pass, have, O_OFF + pass * X_SZ, res, xin + X_SZ, stin + ST_SZ, eb + 1, ph + 1); }
    {
      int ln = lane; asm volatile("" : "+v"(ln));
      const int h = ln >> 5, gi = (ln >> 4) & 1, q = (ln & 15) >> 2, pp = ln & 3, tok = ln & 31;
      CDECL();
      v4u xf[32]; LnSteps lns; if (AB & 2) { lns.rstd = 1.f; lns.nmean = 0.f; }
      const int xb = opq(xin + X_SZ + tb * 32768), sb = opq(stin + ST_SZ + tb * 4096);
      const LB bE(L + (4 * h + q) * 192 + 8 * pp + gi * (3072 + 32)), bO(L + (4 * h + q) * 192 + 8 * pp + gi * (3072 - 32));
      f32x16 acc[6];
#pragma unroll
      for (int c = 0; c < 6; ++c)
#pragma unroll
        for (int i = 0; i < 16; ++i) acc[c][i] = 0.f;
      constexpr int PF = PFD;
      s16x8 wf[PF];
      auto rd = [&](auto N) -> s16x8 { constexpr int s = N.v / 6, u = N.v % 6; LAS char* b = ((u & 1) ? bO : bE).template at<slot_of(pos3(s))>() + 32 * u; return cat(trr(b), trr(b + 1536)); };
      const unsigned vq = ln == 0 ? vsw + (SY_CONS - SY_OFF) : vs + (SY_DUMMY - SY_OFF) + 4 * ln;
      wait_landed(vs, have, pb + pos3(4));
      sfor<0, PF>([&](auto N) { wf[N.v] = rd(N); });
      SBAR();
      spin_ge<SY_READY>(vs, eb + 2);
      v2u pl; unsigned pc;
      STAMP(16 + 4 * (ph + 2));
      CST(3, 0);
      sfor<0, XW>([&](auto S) { xf[S.v] = ldf<S.v>(rs, ln * 16, xb); });
      SBAR();
      sfor<0, 192>([&](auto N) {
        constexpr int n = N.v;
        const s16x8 cur = wf[n % PF];
        if constexpr (n + PF < 192) {
          if constexpr ((n + PF) % 12 == 2 && n + PF > 12) sigq(vq, pb + pos3((n + PF) / 6));
          if constexpr (!(AB & 8) && (n + PF) % 24 == 14 && n + PF < 24 * 7) pl = peek_landed(vs);
          if constexpr (!(AB & 8) && (n + PF) % 24 == 20 && n + PF < 24 * 7) take_landed(have, pl);
          if constexpr (n == 184) pc = peek(vs + 4 * (ln & 1), SY_CS);
          if constexpr (!(AB & 8) && (n + PF) % 24 == 22 && n + PF < 24 * 7) wait_landed(vs, have, pb + pos3((n + PF) / 6 + 5));
          wf[n % PF] = rd(IC<n + PF>{});
        }
        acc[n % 6] = mfma32(cur, xf[n / 6], acc[n % 6]);
        {
          constexpr int st_ = n / 6, k = n % 6 == 0 ? XW + st_ + (st_ < E3 ? st_ : E3) : (n % 6 == 4 && st_ < E3 ? XW + 2 * st_ + 1 : 32);
          if constexpr (k < 32) { if constexpr (AB & 1) xf[k] = xf[k % 8]; else xf[k] = ldf<k>(rs, ln * 16, xb); }
        }
        if constexpr (!(AB & 2) && n % 6 == 2 && n / 6 < 8) lns.st[n / 6] = ldx2(rs, h * 2048 + tok * 8 + (n / 6) * 256, sb);
        if constexpr (!(AB & 2) && n >= 90 && n % 3 == 0 && (n - 90) / 3 < LnSteps::N) lns.template step<(n - 90) / 3>();
        if constexpr (n == 5) CST(3, 1); if constexpr (n == 47) CST(3, 2); if constexpr (n == 95) CST(3, 3); if constexpr (n == 119) CST(3, 4); if constexpr (n == 143) CST(3, 5); if constexpr (n == 167) CST(3, 6); if constexpr (n == 191) CST(3, 7);
        SBAR();
      });
      asm volatile("" : "+v"(acc[0]), "+v"(acc[1]), "+v"(acc[2]), "+v"(acc[3]), "+v"(acc[4]), "+v"(acc[5]));
      sig<SY_CONS>(vsw, pb + P_W4);
      CDUMP(3);
      STAMP(17 + 4 * (ph + 2));
      need_all<SY_CS>(pc, vs + 4 * (ln & 1), 2u);
      const f32x2 rstd = sp(lns.rstd), rstd2 = sp(lns.rstd * lns.rstd), nmean = sp(lns.nmean);
      const int us = (j / 3) * 16 + (j % 3) * 6, nu = (j % 3 == 2) ? 4 : 6;
      const int ho = H_OFF + pass * H_SZ + (tb * 86 + us) * 1024 + ln * 16;
#pragma unroll
      for (int u = 0; u < 6; ++u) {
        if (u < 4 || nu == 6) {
          unsigned hv[4];
#pragma unroll
          for (int g = 0; g < 2; ++g) {
            const f32x4 cg = *(LAS f32x4*)(L + CS3_OFF + (32 * u + 8 * g + 4 * h) * 4), cu = *(LAS f32x4*)(L + CS3_OFF + (32 * u + 16 + 8 * g + 4 * h) * 4);
#pragma unroll
            for (int i = 0; i < 2; ++i) {
              const f32x2 tg = fma2(f32x2{cg[2 * i], cg[2 * i + 1]}, nmean, f32x2{acc[u][4 * g + 2 * i], acc[u][4 * g + 2 * i + 1]});
              const f32x2 tu = fma2(f32x2{cu[2 * i], cu[2 * i + 1]}, nmean, f32x2{acc[u][8 + 4 * g + 2 * i], acc[u][8 + 4 * g + 2 * i + 1]});
              const f32x2 gv = tg * rstd;
              const f32x2 z = gv * fma2(gv * gv, sp(-0.1029432397f), sp(-2.3022081985f));
              const f32x2 d = f32x2{__builtin_amdgcn_exp2f(z.x), __builtin_amdgcn_exp2f(z.y)} + sp(1.f);
              hv[2 * g + i] = pk((tg * rstd2) * tu * f32x2{__builtin_amdgcn_rcpf(d.x), __builtin_amdgcn_rcpf(d.y)});
            }
          }
          stx(v4u{hv[0], hv[1], hv[2], hv[3]}, rs, ho + u * 1024);
          if (u == 3) { asm volatile("s_waitcnt vmcnt(1)" ::: "memory"); sig<SY_PROG>(vsw, eb + 3); }
        }
        SBAR();
      }
      res = ld_resid(rs, ln, wave, t, j, xin + X_SZ);
      STAMP(18 + 4 * (ph + 2));
      hold<3>(vs, eb + 4);
      publish(vsw, eb + 4);
      STAMP(19 + 4 * (ph + 2));
    }
    { int ln = lane; asm volatile("" : "+v"(ln));
      proj_phase<43, true>(p, L, vs, rs, ln, wave, t, j, pass, have, H_OFF + pass * H_SZ, res, xin + 2 * X_SZ, stin + 2 * ST_SZ, eb + 3, ph + 3); }
  }
}

__global__ void __launch_bounds__(512, 2) fused_kernel(Params p) {
  extern __shared__ __attribute__((aligned(16))) char smem[];
  LAS char* L = (LAS char*)smem;
  const int lane = threadIdx.x & 63, wave = __builtin_amdgcn_readfirstlane(threadIdx.x >> 6);
  const int b = blockIdx.x;
  const int t = (b & 7) * 2 + (b >> 7), j = (b >> 3) & 15;
  asm volatile(".subsection 2\n\t.fill 128, 4, 0xBF800000\n\t.subsection 0");
  if (threadIdx.x < 32) *(LAS unsigned*)(L + SY_OFF + threadIdx.x * 4) = 0u;
  __syncthreads();
  const unsigned lb = (unsigned)(size_t)L;
  unsigned vs = lb + SY_OFF; asm volatile("" : "+v"(vs));
  if (wave < 4) { __builtin_amdgcn_s_setprio(2); compute_main(p, L, vs, lane, wave, t, j); }
  else if (wave == 4) { __builtin_amdgcn_s_setprio(3); poller_main(p, L, vs, lane, t, j, wave); }
  else if (wave == 5) helper_main(p, L, vs, lane, j);
  else {
    __builtin_amdgcn_s_setprio(3);
    loader_main(p, lb, lane, j, wave - 6, wave); }
  PROF_DUMP();
}
}

extern "C" void kernel_launch(void* const* d_in, const int* in_sizes, int n_in, void* d_out, int out_size, void* d_ws, size_t ws_size, hipStream_t stream) {
  Params p;
  p.x = (const u16*)d_in[0]; p.wqkv = (const u16*)d_in[1]; p.wo = (const u16*)d_in[2];
  p.wg = (const u16*)d_in[4]; p.wu = (const u16*)d_in[5]; p.wd = (const u16*)d_in[6];
  p.out = (u16*)d_out; p.ws = (unsigned char*)d_ws;
  (void)hipFuncSetAttribute((const void*)fused_kernel, hipFuncAttributeMaxDynamicSharedMemorySize, LDS_ALLOC);
  hipLaunchKernelGGL(fused_kernel, dim3(256), dim3(512), LDS_ALLOC, stream, p);
}
```

```cpp
#include <hip/hip_runtime.h>
#include <hip/hip_bf16.h>

#define GAS __attribute__((address_space(1)))
#define LAS __attribute__((address_space(3)))
#define DI __device__ __forceinline__
typedef unsigned v4u __attribute__((ext_vector_type(4)));
typedef unsigned v2u __attribute__((ext_vector_type(2)));
typedef float f32x2 __attribute__((ext_vector_type(2)));
typedef float f32x4 __attribute__((ext_vector_type(4)));
typedef float f32x16 __attribute__((ext_vector_type(16)));
typedef short s16x4 __attribute__((ext_vector_type(4)));
typedef short s16x8 __attribute__((ext_vector_type(8)));
typedef __bf16 bf16x8 __attribute__((ext_vector_type(8)));
typedef __bf16 bf16x2 __attribute__((ext_vector_type(2)));
typedef unsigned long long u64;
typedef unsigned short u16;
#define RLX_AGENT __ATOMIC_RELAXED, __HIP_MEMORY_SCOPE_AGENT

namespace {
constexpr int AUX_SC1 = 16;
constexpr int FLAG_OFF = 0;
constexpr int EPOCH_OFF = 65536;
constexpr int CSF_OFF = 131072;
constexpr int CSG_OFF = 196608;
constexpr int CSP_OFF = 262144;
constexpr int X_OFF = 1 << 20, X_SZ = 2 << 20;
constexpr int O_OFF = X_OFF + 4 * X_SZ;
constexpr int ST_OFF = O_OFF + 2 * X_SZ, ST_SZ = 64 * 16 * 256;
constexpr int H_OFF = ST_OFF + 4 * ST_SZ, H_SZ = 64 * 86 * 1024;
constexpr int WT1_OFF = H_OFF + 2 * H_SZ, WT3_OFF = WT1_OFF + 16 * 96 * 1024, WT2_OFF = WT3_OFF + 16 * 192 * 1024, WT4_OFF = WT2_OFF + 8 * 64 * 1024;
constexpr int WS_BYTES = WT4_OFF + 8 * 172 * 1024;
constexpr int PROF_OFF = 32 << 20;
static_assert(WS_BYTES <= PROF_OFF, "");
constexpr int NR = 136;
constexpr int EX_OFF = NR * 1024, PLANE = 144 * 16;
constexpr int CS1_OFF = EX_OFF + 8 * PLANE, CS3_OFF = CS1_OFF + 96 * 4;
constexpr int LN_OFF = CS3_OFF + 192 * 4;
constexpr int KP_OFF = 79 * 1024;
constexpr int SY_OFF = LN_OFF + 16 * 8;
constexpr int SY_LANDED = SY_OFF;
constexpr int SY_CONS = SY_OFF + 16;
constexpr int SY_WT = SY_OFF + 32, SY_RAW = SY_OFF + 144;
constexpr int SY_HOLD = SY_OFF + 40, SY_READY = SY_OFF + 44;
constexpr int SY_PROG = SY_OFF + 48;
constexpr int SY_KV = SY_OFF + 64;
constexpr int SY_XF = SY_OFF + 80;
constexpr int SY_CS = SY_OFF + 96;
constexpr int SY_T4 = SY_OFF + 104;
constexpr int SY_LN = SY_OFF + 112, SY_WD = SY_OFF + 116;
constexpr int SY_TC = SY_OFF + 128;
constexpr int SY_DUMMY = SY_OFF + 160;
constexpr int LDS_BYTES = SY_DUMMY + 256;
constexpr int P_W1 = 0, P_W2 = 96, P_W3 = 160, P_W4 = 352, P_END = 544;
constexpr int QA = 24;
constexpr int pos1(int s, int c  ) { return s < QA ? 3 * s + c : (c < 2 ? 3 * QA + 2 * (s - QA) + c : 64 + s); }
constexpr int pos2(int a) { return P_W2 + 4 * a; }
constexpr int pos3(int s) { return P_W3 + 6 * s; }
constexpr int pos4(int a) { return P_W4 + 4 * a; }
constexpr int T4 = 34;
constexpr int ustart(int j) { return (j / 3) * 16 + (j % 3) * 6; }
constexpr bool early_unit(int kh, int u) { for (int i = 0; i < 8; ++i) if (u >= ustart(8 * kh + i) && u < ustart(8 * kh + i) + 3) return true; return false; }
constexpr int ord4(int kh, int a) {
  if (a < 24) return ustart(8 * kh + a / 3) + a % 3;
  int n = 24;
  for (int u = 43 * kh; u < 43 * kh + 43; ++u) if (!early_unit(kh, u)) { if (n == a) return u; ++n; }
  return -1;
}
static_assert(ord4(0, 42) == 42 && ord4(1, 24) == 43 && ord4(1, 42) == 85, "unit order");
constexpr int slot_of(int p) { return (p % NR) * 1024; }

struct Params { const u16* x; const u16* wqkv; const u16* wo; const u16* wg; const u16* wu; const u16* wd; u16* out; unsigned char* ws; };

DI unsigned rfl(unsigned v) { return __builtin_amdgcn_readfirstlane(v); }
DI u64 uni64(u64 e) { return ((u64)rfl((unsigned)(e >> 32)) << 32) | rfl((unsigned)e); }
DI float asf(unsigned u) { return __builtin_bit_cast(float, u); }
DI unsigned asu(float f) { return __builtin_bit_cast(unsigned, f); }
DI unsigned pk(float a, float b) { bf16x2 v = {(__bf16)a, (__bf16)b}; return __builtin_bit_cast(unsigned, v); }
DI float bflo(unsigned u) { return asf(u << 16); }
DI float bfhi(unsigned u) { return asf(u & 0xffff0000u); }
DI float rbf(float a) { return bflo(pk(a, a)); }
DI float dot2(unsigned a, unsigned b, float c) { return __builtin_amdgcn_fdot2_f32_bf16(__builtin_bit_cast(bf16x2, a), __builtin_bit_cast(bf16x2, b), c, false); }
DI float dot2z(unsigned a, unsigned b) { float r; asm("v_dot2_f32_bf16 %0, %1, %2, 0" : "=v"(r) : "v"(a), "v"(b)); return r; }
DI float max3(float a, float b, float c) { float r; asm("v_max3_f32 %0, %1, %2, %3" : "=v"(r) : "v"(a), "v"(b), "v"(c)); return r; }
DI float add1(float a, float b) { float r; asm("v_add_f32 %0, %1, %2" : "=v"(r) : "v"(a), "v"(b)); return r; }
DI float add3(float a, float b, float c) { return __builtin_constant_p(c) ? add1(a, b) : add1(add1(a, b), c); }
DI f32x2 fma2(f32x2 a, f32x2 b, f32x2 c) { return __builtin_elementwise_fma(a, b, c); }
DI f32x2 sp(float a) { return f32x2{a, a}; }
DI unsigned pk(f32x2 v) { return pk(v.x, v.y); }
template <int I> DI f32x2 pr(const f32x16& v) { return f32x2{v[2 * I], v[2 * I + 1]}; }
DI float sum32(float x) { auto r = __builtin_amdgcn_permlane32_swap(asu(x), asu(x), false, false); return asf(r[0]) + asf(r[1]); }
DI float sum16(float x) { auto r = __builtin_amdgcn_permlane16_swap(asu(x), asu(x), false, false); return asf(r[0]) + asf(r[1]); }
DI unsigned ror8(unsigned x) { return (unsigned)__builtin_amdgcn_update_dpp(0, (int)x, 0x128, 0xf, 0xf, false); }
DI f32x16 mfma32(s16x8 a, v4u b, f32x16 c) { return __builtin_amdgcn_mfma_f32_32x32x16_bf16(__builtin_bit_cast(bf16x8, a), __builtin_bit_cast(bf16x8, b), c, 0, 0, 0); }
DI f32x4 mfma16(s16x8 a, v4u b, f32x4 c) { return __builtin_amdgcn_mfma_f32_16x16x32_bf16(__builtin_bit_cast(bf16x8, a), __builtin_bit_cast(bf16x8, b), c, 0, 0, 0); }
DI s16x4 trr(LAS char* p) { return __builtin_amdgcn_ds_read_tr16_b64_v4i16((LAS s16x4*)p); }
DI s16x8 cat(s16x4 a, s16x4 b) { return __builtin_shufflevector(a, b, 0, 1, 2, 3, 4, 5, 6, 7); }
#define CBAR() asm volatile("" ::: "memory")
#define SBAR() __builtin_amdgcn_sched_barrier(0)
#define DRAIN() asm volatile("s_waitcnt vmcnt(0)" ::: "memory")

#define STAMP(i) do {} while (0)
#define PROF_DUMP() do {} while (0)
constexpr int LDS_ALLOC = LDS_BYTES;

#define CDECL() do {} while (0)
#define CST(site, i) do {} while (0)
#define CDUMP(site) do {} while (0)
template <int I> struct IC { static constexpr int v = I; };
template <int B, int E, class F> DI void sfor(F&& f) { if constexpr (B < E) { f(IC<B>{}); sfor<B + 1, E>(f); } }
DI int opq(int x) { asm volatile("" : "+s"(x)); return x; }
DI LAS char* opqv(LAS char* x) { asm volatile("" : "+v"(x)); return x; }

template <int OFF> DI void spin_ge(unsigned vs, unsigned need) {
  unsigned tv, ts, cnt;
  asm volatile("s_mov_b32 %2, 0x40000\n.Lsp%=:\n\tds_read_b32 %0, %3 offset:%5\n\ts_waitcnt lgkmcnt(0)\n\tv_readfirstlane_b32 %1, %0\n\t"
               "s_cmp_ge_u32 %1, %4\n\ts_cbranch_scc1 .Lsd%=\n\ts_sub_u32 %2, %2, 1\n\ts_cbranch_scc0 .Lsp%=\n.Lsd%=:"
               : "=&v"(tv), "=&s"(ts), "=&s"(cnt) : "v"(vs), "s"(need), "i"(OFF - SY_OFF) : "memory", "scc");
}
template <int OFF> DI void spin_all_ge(unsigned vs4, unsigned need) {
  unsigned tv, cnt;
  asm volatile("s_mov_b32 %1, 0x40000\n.Lsp%=:\n\tds_read_b32 %0, %2 offset:%4\n\ts_waitcnt lgkmcnt(0)\n\tv_cmp_gt_u32 vcc, %3, %0\n\t"
               "s_cbranch_vccz .Lsd%=\n\ts_sub_u32 %1, %1, 1\n\ts_cbranch_scc0 .Lsp%=\n.Lsd%=:"
               : "=&v"(tv), "=&s"(cnt) : "v"(vs4), "s"(need), "i"(OFF - SY_OFF) : "memory", "scc", "vcc");
}
DI void wait_landed(unsigned vs, unsigned& have, unsigned need) {
  unsigned t0, t1, cnt;
  asm volatile("s_cmp_ge_u32 %0, %5\n\ts_cbranch_scc0 .Lslow%=\n.Lback%=:\n\t.subsection 1\n.Lslow%=:\n\ts_mov_b32 %3, 0x40000\n.Lsp%=:\n\t"
               "ds_read_b32 %1, %4 offset:%6\n\tds_read_b32 %2, %4 offset:%6+4\n\ts_waitcnt lgkmcnt(0)\n\tv_min_u32 %1, %1, %2\n\ts_nop 0\n\tv_readfirstlane_b32 %0, %1\n\t"
               "s_cmp_ge_u32 %0, %5\n\ts_cbranch_scc1 .Lback%=\n\ts_sub_u32 %3, %3, 1\n\ts_cbranch_scc0 .Lsp%=\n\ts_branch .Lback%=\n\t.subsection 0"
               : "+s"(have), "=&v"(t0), "=&v"(t1), "=&s"(cnt) : "v"(vs), "s"(need), "i"(SY_LANDED - SY_OFF) : "memory", "scc");
}
template <int OFF> DI void sig(unsigned vs, unsigned val) {
  asm volatile("s_mov_b64 exec, 1\n\tds_write_b32 %0, %1 offset:%2\n\ts_mov_b64 exec, -1" :: "v"(vs), "v"(val), "i"(OFF - SY_OFF) : "memory");
}
DI void sigq(unsigned vq, unsigned val) { asm volatile("ds_write_b32 %0, %1" :: "v"(vq), "v"(val) : "memory"); }

DI unsigned peek(unsigned addr, int off) { return *(volatile LAS unsigned*)(size_t)(addr + off - SY_OFF); }
template <int OFF> DI void need_all(unsigned seen, unsigned vs4, unsigned need) {
  unsigned tv, cnt;
  asm volatile("v_cmp_gt_u32 vcc, %3, %5\n\ts_cbranch_vccnz .Lslow%=\n.Lback%=:\n\t.subsection 1\n.Lslow%=:\n\ts_mov_b32 %1, 0x40000\n.Lsp%=:\n\t"
               "ds_read_b32 %0, %2 offset:%4\n\ts_waitcnt lgkmcnt(0)\n\tv_cmp_gt_u32 vcc, %3, %0\n\t"
               "s_cbranch_vccz .Lback%=\n\ts_sub_u32 %1, %1, 1\n\ts_cbranch_scc0 .Lsp%=\n\ts_branch .Lback%=\n\t.subsection 0"
               : "=&v"(tv), "=&s"(cnt) : "v"(vs4), "s"(need), "i"(OFF - SY_OFF), "v"(seen) : "memory", "scc", "vcc");
}
DI v2u peek_landed(unsigned vs) { return *(volatile LAS v2u*)(size_t)(vs + SY_LANDED - SY_OFF); }
DI void take_landed(unsigned& have, v2u v) { asm volatile("s_max_u32 %0, %0, %1" : "+s"(have) : "s"(rfl(min(v.x, v.y))) : "scc"); }
DI void publish(unsigned vsw, unsigned ev) { DRAIN(); sig<SY_PROG>(vsw, ev); }

DI v4u ln_stats(const v4u (&st)[4]) {
  f32x2 s = {0.f, 0.f};
#pragma unroll
  for (int i = 0; i < 4; ++i) s += f32x2{asf(st[i].x), asf(st[i].z)};
  const f32x2 mean = f32x2{sum32(sum16(s.x)), sum32(sum16(s.y))} * sp(1.f / 512.f);
  f32x2 m2 = {0.f, 0.f};
#pragma unroll
  for (int i = 0; i < 4; ++i) { const f32x2 d = fma2(f32x2{asf(st[i].x), asf(st[i].z)}, sp(1.f / 32.f), -mean); m2 += fma2(d * sp(32.f), d, f32x2{asf(st[i].y), asf(st[i].w)}); }
  const float v0 = sum32(sum16(m2.x)) * (1.f / 512.f), v1 = sum32(sum16(m2.y)) * (1.f / 512.f);
  const float sd0 = rbf(__builtin_amdgcn_sqrtf(rbf(rbf(v0) + 1e-5f))), sd1 = rbf(__builtin_amdgcn_sqrtf(rbf(rbf(v1) + 1e-5f)));
  return v4u{asu(__builtin_amdgcn_rcpf(sd0)), asu(-rbf(mean.x)), asu(__builtin_amdgcn_rcpf(sd1)), asu(-rbf(mean.y))};
}
struct LnSteps {
  v2u st[8]; f32x2 a, b, c, d; float S, R, m16, rstd, nmean, sd;
  static constexpr int N = 22;
  DI f32x2 f(int i) const { return f32x2{asf(st[i].x), asf(st[i].y)}; }
  template <int I> DI void step() {
    if constexpr (I == 0) { a.x = add3(asf(st[0].x), asf(st[1].x), asf(st[2].x)); b.x = add3(asf(st[3].x), asf(st[4].x), asf(st[5].x)); }
    else if constexpr (I == 1) { a.y = add3(asf(st[0].y), asf(st[1].y), asf(st[2].y)); b.y = add3(asf(st[3].y), asf(st[4].y), asf(st[5].y)); }
    else if constexpr (I == 2) { a.x = add3(add3(a.x, asf(st[6].x), asf(st[7].x)), b.x, 0.f); }
    else if constexpr (I == 3) { a.y = add3(add3(a.y, asf(st[6].y), asf(st[7].y)), b.y, 0.f); }
    else if constexpr (I == 4) { S = sum32(a.x); }
    else if constexpr (I == 5) { m16 = S * (1.f / 16.f); nmean = S * (-1.f / 512.f); }
    else if constexpr (I == 6) { const float e = asf(st[0].x) - m16; R = e * e; }
    else if constexpr (I < 14) { const float e = asf(st[I - 6].x) - m16; R = __builtin_fmaf(e, e, R); }
    else if constexpr (I == 14) { R = __builtin_fmaf(R, 1.f / 32.f, a.y); }
    else if constexpr (I == 15) { R = sum32(R); }
    else if constexpr (I == 16) { R = rbf(R * (1.f / 512.f)); }
    else if constexpr (I == 17) { R = rbf(R + 1e-5f); }
    else if constexpr (I == 18) { R = __builtin_amdgcn_sqrtf(R); }
    else if constexpr (I == 19) { R = rbf(R); }
    else if constexpr (I == 20) { sd = R; rstd = __builtin_amdgcn_rcpf(R); }
    else if constexpr (I == 21) { nmean = rbf(nmean); }

    if constexpr (I == 4) asm volatile("" : "+v"(S)); if constexpr (I == 5) asm volatile("" : "+v"(m16), "+v"(nmean));
    if constexpr (I >= 6 && I < 20) asm volatile("" : "+v"(R)); if constexpr (I == 20) asm volatile("" : "+v"(rstd)); if constexpr (I == 21) asm volatile("" : "+v"(nmean));
  }
};
DI v4u ldx(__amdgpu_buffer_rsrc_t rs, int voff, int soff) { return __builtin_amdgcn_raw_buffer_load_b128(rs, voff, soff, AUX_SC1); }
DI v2u ldx2(__amdgpu_buffer_rsrc_t rs, int voff, int soff) { return __builtin_amdgcn_raw_buffer_load_b64(rs, voff, soff, AUX_SC1); }

#define MAPB 0
#define AB 0
#define DEPTH1 20
#define NE1 0
#define W1GATE SY_SWEPT
DI void stx(v4u v, __amdgpu_buffer_rsrc_t rs, int voff) { __builtin_amdgcn_raw_buffer_store_b128(v, rs, voff, 0, AUX_SC1); }
DI void stx2(v2u v, __amdgpu_buffer_rsrc_t rs, int voff) { __builtin_amdgcn_raw_buffer_store_b64(v, rs, voff, 0, AUX_SC1); }
#define CS3E 8
#define G2WORD SY_GO2
#define E1 8
#define E3 8
#define PFD 6
#define LATE0 28
#define XW 8
#define DEPTH 12
struct Loader {
  unsigned vsl  , vs6  , vs, cons; int pub;
#define LOOK "ds_read_b32 %3, %4 offset:%7\n\ts_waitcnt lgkmcnt(0)\n\t" \
             "v_min_u32_dpp %3, %3, %3 row_shr:2 row_mask:0xf bank_mask:0xf\n\ts_nop 1\n\t" \
             "v_min_u32_dpp %3, %3, %3 row_shr:1 row_mask:0xf bank_mask:0xf\n\ts_nop 0\n\t" \
             "v_readlane_b32 %0, %3, 3\n\ts_cmp_ge_u32 %0, %5\n\ts_cbranch_scc1 .Lback%=\n\t"
#define RUNG(N) "s_waitcnt vmcnt(" #N ")\n\ts_sub_i32 %1, %8, 2 * " #N "\n\ts_max_i32 %2, %2, %1\n\tv_mov_b32 %3, %2\n\tds_write_b32 %6, %3\n\t" LOOK
  unsigned tmp;
  DI void room(unsigned pos, unsigned end) {
    unsigned cnt;
    asm volatile("s_cmp_ge_i32 %0, %5\n\ts_cbranch_scc0 .Lslow%=\n.Lback%=:\n\t.subsection 1\n.Lslow%=:\n\t" LOOK RUNG(8) RUNG(4)
                 "s_waitcnt vmcnt(0)\n\tv_mov_b32 %3, %8\n\ts_mov_b32 %2, %8\n\tds_write_b32 %6, %3\n\ts_mov_b32 %1, 0x40000\n.Lsp%=:\n\t" LOOK
                 "s_sleep 1\n\ts_sub_u32 %1, %1, 1\n\ts_cbranch_scc0 .Lsp%=\n\ts_branch .Lback%=\n\t.subsection 0"
                 : "+s"(cons), "=&s"(cnt), "+s"(pub) : "v"(tmp), "v"(vs6), "s"(end - NR), "v"(vsl), "i"(SY_CONS - SY_OFF), "s"(pos) : "memory", "scc");
  }
  unsigned hv, rv;
  DI void look() { asm volatile("ds_read_b32 %0, %2 offset:%3\n\tds_read_b32 %1, %2 offset:%4" : "=&v"(hv), "=&v"(rv) : "v"(vs), "i"(SY_HOLD - SY_OFF), "i"(SY_READY - SY_OFF) : "memory"); }
  DI void done(unsigned end) {
    pub = max((int)end - 2 * DEPTH, pub);
    unsigned cnt;
    asm volatile("s_waitcnt vmcnt(%7) lgkmcnt(0)\n\tds_write_b32 %4, %5\n\tv_cmp_gt_u32 vcc, %2, %3\n\ts_cbranch_vccnz .Lslow%=\n.Lback%=:\n\t.subsection 1\n.Lslow%=:\n\t"
                 "s_waitcnt vmcnt(0)\n\tv_mov_b32 %2, %6\n\ts_mov_b32 %0, %6\n\tds_write_b32 %4, %2\n\ts_mov_b32 %1, 0x40000\n.Lsp%=:\n\t"
                 "ds_read_b32 %2, %8 offset:%9\n\tds_read_b32 %3, %8 offset:%10\n\ts_waitcnt lgkmcnt(0)\n\tv_cmp_gt_u32 vcc, %2, %3\n\ts_cbranch_vccz .Lback%=\n\t"
                 "s_sub_u32 %1, %1, 1\n\ts_cbranch_scc0 .Lsp%=\n\t"
                 "s_branch .Lback%=\n\t.subsection 0"
                 : "+s"(pub), "=&s"(cnt) : "v"(hv), "v"(rv), "v"(vsl), "v"(pub), "s"(end), "i"(DEPTH), "v"(vs), "i"(SY_HOLD - SY_OFF), "i"(SY_READY - SY_OFF) : "memory", "scc", "vcc");
  }
};
#define GL0(v, rs, so, c) "buffer_load_dwordx4 " v ", " rs ", " so " offen" c " lds\n\t"
#define GL1(v, rs, so, c) "s_nop 0\n\t" GL0(v, rs, so, c)
#define GLN(v, rs, so, c) "s_add_u32 m0, m0, 0x800\n\t" GL1(v, rs, so, c)
DI v4u srd(const void* base, unsigned bytes) { const u64 a = (u64)base; return v4u{(unsigned)a, (unsigned)(a >> 32) & 0xFFFFu, bytes, 0x00020000u}; }
DI void dma1(unsigned lds, unsigned voff, v4u r, unsigned so) { asm volatile("s_mov_b32 m0, %0\n\t" GL1("%1", "%2", "%3", "") :: "s"(lds), "v"(voff), "s"(r), "s"(so) : "memory"); }
DI int wg_of(int tt, int jj) { return ((tt & 1) * 16 + jj) * 8 + (tt >> 1); }
DI void frag_out(s16x8 f, __amdgpu_buffer_rsrc_t rs, int off, float& cs) {
  const v4u v = __builtin_bit_cast(v4u, f); const unsigned ones = 0x3f803f80u;
  stx(v, rs, off);
  cs = dot2(v.w, ones, dot2(v.z, ones, dot2(v.y, ones, dot2(v.x, ones, cs))));
}
constexpr int STG1 = 96 * 1024, STG3 = 102 * 1024, STG2 = 114 * 1024;

DI void loader_main(const Params& p, LAS char* L, unsigned lb, int lane, int t, int j, int l, int wave) {
  const __amdgpu_buffer_rsrc_t rs = __builtin_amdgcn_make_buffer_rsrc((void*)p.ws, 0, WS_BYTES, 0x00020000);
  Loader ld; ld.vsl = lane == 0 ? lb + SY_LANDED + 4 * l : lb + SY_DUMMY + 4 * lane; ld.vs6 = lb + SY_OFF + 4 * (lane & 3);
  ld.cons = opq(0); ld.pub = opq(0); ld.vs = lb + SY_OFF; asm volatile("v_mov_b32 %0, 0" : "=v"(ld.tmp));
  auto fresh = [&]() { int x = lane; asm volatile("" : "+v"(x)); return x; };
  const v4u rw = srd(p.ws, WS_BYTES);
  const unsigned lbl = lb + l * 1024;
  auto seg = [&](unsigned so, unsigned pos, int n) {
    const unsigned v0 = fresh() * 16 + l * 1024;
    unsigned m = lbl + (pos % NR) * 1024;
#pragma nounroll
    for (int i = 0; i < n; ++i) {
      ld.room(pos, pos + 8);
      ld.look();
      asm volatile("s_mov_b32 m0, %0\n\t" GL1("%1", "%5", "%6", " sc1") GLN("%2", "%5", "%6", " sc1") GLN("%3", "%5", "%6", " sc1") GLN("%4", "%5", "%6", " sc1")
                   :: "s"(m), "v"(v0), "v"(v0 + 2048), "v"(v0 + 4096), "v"(v0 + 6144), "s"(rw), "s"(so) : "memory", "scc");
      pos += 8; so += 8192; m += 8192; if (m >= lbl + NR * 1024) m -= NR * 1024;
      ld.done(pos);
    }
  };
  v4u w1[8][6];
  LAS char* L1; LAS char* L1b;
  auto base1 = [&]() { L1 = (LAS char*)(size_t)(lb + fresh() * 16 + l * 6144); L1b = opqv(L1 + 49152); };
  {
    spin_ge<SY_WT>(ld.vs, 1);
    STAMP(16);
    const unsigned v0 = fresh() * 16 + l * 1024;
    unsigned m = lbl, so = WT1_OFF + j * 96 * 1024;
#pragma nounroll
    for (int i = 0; i < 12; ++i) {
      asm volatile("s_mov_b32 m0, %0\n\t" GL1("%1", "%5", "%6", " sc1") GLN("%2", "%5", "%6", " sc1") GLN("%3", "%5", "%6", " sc1") GLN("%4", "%5", "%6", " sc1")
                   "s_waitcnt vmcnt(%7)\n\tds_write_b32 %8, %9"
                   :: "s"(m), "v"(v0), "v"(v0 + 2048), "v"(v0 + 4096), "v"(v0 + 6144), "s"(rw), "s"(so), "i"(DEPTH1), "v"(ld.vsl), "v"(max(8 * i + 8 - 2 * DEPTH1, 0)) : "memory", "scc");
      so += 8192; m += 8192;
    }
    STAMP(17);
  }
  {
    const int jc = j >> 1, idx = 2 * t + (j & 1);
    const v4u ro = srd(p.wo, 512 * 1024), rd = srd(p.wd, 1376 * 1024);
    const unsigned o2 = (lane >> 3) * 1024 + (((lane & 7) ^ (((lane >> 4) & 1) * 4)) * 16) + 128 * jc;
    const v4u ra = l ? rd : ro;
#pragma unroll
    for (int i = 0; i < 4; ++i) dma1(lb + STG2 + l * 4096 + i * 1024, o2 + (i & 1) * 8192, i < 2 ? ra : rd, (idx + 32 * (i < 2 ? l : 2 * l)) * 16384);
    asm volatile("s_waitcnt vmcnt(16)" ::: "memory"); sigq(ld.vsl, 72);
    asm volatile("s_waitcnt vmcnt(4)" ::: "memory"); sigq(ld.vsl, 73);
    STAMP(20);
    spin_all_ge<SY_LANDED>(ld.vs + 4 * (lane & 1), 73);
    base1();
    sfor<0, 8>([&](auto I) {
#pragma unroll
      for (int q = 0; q < 6; ++q) w1[I.v][q] = *(LAS v4u*)((I.v < 4 ? L1 : L1b) + (I.v & 3) * 12288 + q * 1024);
      SBAR();
    });
    ld.pub = opq(P_W2); sigq(ld.vsl, ld.pub);
    STAMP(21);
    const int h = lane >> 5, gi = (lane >> 4) & 1, q = (lane & 15) >> 2, pp = lane & 3;
    const int o0 = (4 * h + q) * 128 + ((2 * gi + (pp >> 1)) ^ (4 * ((q >> 1) & 1))) * 16 + 8 * (pp & 1);
    DRAIN();
#pragma unroll
    for (int i = 0; i < 2; ++i) {
      const int u = idx + 32 * (i ? 2 * l : l), kh = u >= 43, r16 = u & 15, jp = 3 * (u >> 4) + (r16 >= 12 ? 2 : r16 >= 6), r = r16 - (r16 >= 12 ? 12 : (r16 >= 6 ? 6 : 0)), jr = jp - 8 * kh;
      const int a = (r < 3 && jr >= 0) ? 3 * jr + r : 24 + u - 43 * kh - 3 * (jr + 1);
      const int dst = (i == 0 && l == 0) ? WT2_OFF / 1024 + jc * 64 + 4 * (idx & 15) + 2 * (idx >> 4) : WT4_OFF / 1024 + jc * 172 + 4 * a + 2 * kh;
      if (u < 86) {
#pragma unroll
        for (int c = 0; c < 2; ++c) { LAS char* b = L + STG2 + l * 4096 + i * 2048 + (o0 ^ (64 * c)); stx(__builtin_bit_cast(v4u, cat(trr(b), trr(b + 1024))), rs, (dst + c) * 1024 + lane * 16); }
      }
    }
  }
  DRAIN();
  STAMP(22);
  if (l == 1) { sig<SY_TC>(ld.vs, 1u); spin_ge<SY_WD>(ld.vs, 1); }
  else {
    const u64 E = uni64(__hip_atomic_load((GAS u64*)(p.ws + EPOCH_OFF), RLX_AGENT));
    spin_ge<SY_TC>(ld.vs, 1u);
    if (lane == 0) __hip_atomic_store((GAS u64*)(p.ws + CSG_OFF + blockIdx.x * 256), E + 1, RLX_AGENT);
    GAS u64* f = (GAS u64*)(p.ws + CSG_OFF + wg_of(lane & 15, j ^ ((lane >> 4) & 1)) * 256);
    for (unsigned k = 0; k < (1u << 16); ++k) {
      u64 v = E + 1;
      if (lane < 32) v = __hip_atomic_load(f, RLX_AGENT);
      if (__all(v == E + 1)) break;
    }
    __builtin_amdgcn_fence(__ATOMIC_ACQUIRE, "wavefront");
    sig<SY_WD>(ld.vs, 1u);
  }
  spin_ge<SY_TC + 4>(ld.vs, 1u);
#pragma nounroll
  for (int pass = 0; pass < 2; ++pass) {
    const unsigned pb = pass * P_END;
    STAMP(40 + 8 * pass);
    seg(WT2_OFF + (j >> 1) * 64 * 1024, pb + P_W2, 8);
    STAMP(41 + 8 * pass);
    if (pass == 0) { DRAIN(); ld.pub = opq(P_W3); sigq(ld.vsl, ld.pub); spin_ge<SY_WT>(ld.vs, 2); }
    seg(WT3_OFF + j * 192 * 1024, pb + P_W3, 24);
    STAMP(42 + 8 * pass);
    seg(WT4_OFF + (j >> 1) * 172 * 1024, pb + P_W4, T4 / 2);
    DRAIN(); ld.pub = opq(pb + P_END); sigq(ld.vsl, ld.pub);
    STAMP(43 + 8 * pass);
    if (pass == 1) break;
    base1();
    sfor<0, 8>([&](auto I) {
      const unsigned pos = P_END + 12 * I.v + 6 * l;
      ld.room(pos, pos + 6);
#pragma unroll
      for (int q = 0; q < 6; ++q) *(LAS v4u*)((I.v < 4 ? L1 : L1b) + (I.v & 3) * 12288 + q * 1024) = w1[I.v][q];
      ld.pub = opq(I.v < 7 ? pos + 12 : P_END + P_W2); sigq(ld.vsl, ld.pub);
    });
  }
}

struct Tail {
  v4u r[2 * (43 - T4)];
  DI void load(__amdgpu_buffer_rsrc_t rs, unsigned vs, int lane, int j, int e) {
    spin_ge<SY_WD>(vs, 1);
#pragma unroll
    for (int i = 0; i < 2 * (43 - T4); ++i) r[i] = ldx(rs, lane * 16 + (i & 1) * 1024, WT4_OFF + ((j >> 1) * 172 + 4 * (T4 + (i >> 1)) + 2 * e) * 1024);
    SBAR();
  }
  DI void put(LAS char* L, unsigned vs, int lane, int e, int pass) {
    spin_all_ge<SY_CONS>(vs + 4 * (lane & 3), pass * P_END + pos4(43) - NR);
#pragma unroll
    for (int i = 0; i < 2 * (43 - T4); ++i) *(LAS v4u*)(L + slot_of(pos4(T4)) + (4 * (i >> 1) + (i & 1)) * 1024 + e * 2048 + lane * 16) = r[i];
    sig<SY_T4>(vs + 4 * e, pass + 1);
  }
};
DI void helper_main(const Params& p, LAS char* L, unsigned lb, unsigned vs, int lane, int t, int j) {
  const int wave = 5; (void)wave;
  const __amdgpu_buffer_rsrc_t rs = __builtin_amdgcn_make_buffer_rsrc((void*)p.ws, 0, WS_BYTES, 0x00020000);
  const int h = lane >> 5, gi = (lane >> 4) & 1, q = (lane & 15) >> 2, pp = lane & 3;
  {
    const v4u rq = srd(p.wqkv, 512 * 3072);
    const unsigned o1 = (lane >> 2) * 3072 + (lane & 3) * 16 + 64 * j;
#pragma unroll
    for (int i = 0; i < 6; ++i) dma1(lb + STG1 + i * 1024, o1 + (i % 3) * 1024, rq, (2 * t + i / 3) * (16 * 3072));
    const u64 e_ = __hip_atomic_load((GAS u64*)(p.ws + EPOCH_OFF), RLX_AGENT);
    asm volatile("s_waitcnt vmcnt(1)" ::: "memory");
    STAMP(12);
    LAS char* b1 = L + STG1 + (4 * h + q) * 64 + gi * 32 + pp * 8;
#pragma unroll
    for (int i = 0; i < 6; ++i) {
      const int ks = 2 * t + i / 3, c = (i % 3 + 2) % 3;
      const int pos = ks < QA ? 3 * ks + c : (c < 2 ? 3 * QA + 2 * (ks - QA) + c : 64 + ks);
      float cs = 0.f;
      frag_out(cat(trr(b1 + i * 1024), trr(b1 + i * 1024 + 512)), rs, WT1_OFF + (j * 96 + pos) * 1024 + lane * 16, cs);
      cs = sum32(cs);
      if (lane < 32) __builtin_amdgcn_raw_buffer_store_b32(asu(cs), rs, CSP_OFF + ((j * 32 + ks) * 288 + 32 * (i % 3) + lane) * 4, 0, AUX_SC1);
    }
    DRAIN();
    const u64 E = uni64(e_);
    GAS u64* mine = (GAS u64*)(p.ws + CSF_OFF + blockIdx.x * 256);
    if (lane == 0) __hip_atomic_store(mine, E + 1, RLX_AGENT);
    auto poll = [&](GAS u64* f, int n, u64 want) {
      for (unsigned k = 0; k < (1u << 16); ++k) {
        u64 v = want;
        if (lane < n) v = __hip_atomic_load(f, RLX_AGENT);
        if (__all(v - want <= E + 2 - want)) break;
      }
      __builtin_amdgcn_fence(__ATOMIC_ACQUIRE, "wavefront");
    };
    GAS u64* f = (GAS u64*)(p.ws + CSF_OFF + wg_of(lane & 15, j ^ ((lane >> 4) & 1)) * 256);
    poll(f, 16, E + 1);
    sig<SY_WT>(vs, 1u);
    STAMP(13);
    {
      const int l2 = lane < 48 ? lane : lane - 48, hi = l2 >= 24;
      v4u g[16];
#pragma unroll
      for (int i = 0; i < 16; ++i) g[i] = ldx(rs, hi * 1152 + (l2 - 24 * hi) * 16, CSP_OFF + (j * 32 + 2 * i) * 1152);
      SBAR();
      f32x4 tot = __builtin_bit_cast(f32x4, g[0]);
#pragma unroll
      for (int i = 1; i < 16; ++i) tot += __builtin_bit_cast(f32x4, g[i]);
#pragma unroll
      for (int i = 0; i < 4; ++i) tot[i] += asf(__builtin_amdgcn_ds_bpermute((lane + 24) * 4, asu(tot[i])));
      if (lane < 24) *(LAS f32x4*)(L + CS1_OFF + lane * 16) = tot;
      sig<SY_CS>(vs, 1u);
      STAMP(15);
    }
    {
      LAS char* bE = L + STG3 + (4 * h + q) * 192 + 8 * pp + gi * (3072 + 32); LAS char* bO = bE - gi * 64;
      spin_all_ge<SY_RAW>(vs + 4 * (lane & 3), 1u);
#pragma unroll
      for (int i = 0; i < 12; ++i) {
        const int u = i % 6, ks = 2 * t + i / 6;
        LAS char* b = ((u & 1) ? bO : bE) + 32 * u + (i / 6) * 6144; float cs = 0.f;
        frag_out(cat(trr(b), trr(b + 1536)), rs, WT3_OFF + (j * 192 + 6 * ks + u) * 1024 + lane * 16, cs);
        cs = sum32(cs);
        if (lane < 32) __builtin_amdgcn_raw_buffer_store_b32(asu(cs), rs, CSP_OFF + ((j * 32 + ks) * 288 + 96 + 32 * u + lane) * 4, 0, AUX_SC1);
      }
      sig<SY_TC + 4>(vs, 1u);
      DRAIN();
    }
    STAMP(17);
    if (lane == 0) __hip_atomic_store(mine, E + 2, RLX_AGENT);
    poll(f, 16, E + 2);
    sig<SY_WT>(vs, 2u);
    STAMP(14);
    {
      f32x4 tot = {0.f, 0.f, 0.f, 0.f};
#pragma unroll
      for (int k = 0; k < 2; ++k) {
        v4u g[16];
#pragma unroll
        for (int i = 0; i < 16; ++i) g[i] = ldx(rs, 384 + min(lane, 47) * 16, CSP_OFF + (j * 32 + 16 * k + i) * 1152);
        SBAR();
#pragma unroll
        for (int i = 0; i < 16; ++i) tot += __builtin_bit_cast(f32x4, g[i]);
      }
      if (lane < 48) *(LAS f32x4*)(L + CS3_OFF + lane * 16) = tot;
      sig<SY_CS>(vs, 2u);
    }
  }
  spin_ge<SY_READY>(vs, 3u);
  Tail tl; tl.load(rs, vs, lane, j, 1);
  tl.put(L, vs, lane, 1, 0);
  tl.put(L, vs, lane, 1, 1);
}

DI void poller_main(const Params& p, LAS char* L, unsigned vs, int lane, int t, int j, int wave) {
  const __amdgpu_buffer_rsrc_t rs = __builtin_amdgcn_make_buffer_rsrc((void*)p.ws, 0, WS_BYTES, 0x00020000);
  const u64 E = uni64(__hip_atomic_load((GAS u64*)(p.ws + EPOCH_OFF), RLX_AGENT));
  const int own = wg_of(t, lane & 15), prev = wg_of(t > 0 ? t - 1 : 0, lane & 15);
  auto hop = [&](unsigned ev, bool halo) {
    spin_all_ge<SY_PROG>(vs + 4 * (lane & 3), ev);
    if (lane == 0) __hip_atomic_store((GAS u64*)(p.ws + FLAG_OFF + blockIdx.x * 256), E + ev, RLX_AGENT);
    GAS u64* f = (GAS u64*)(p.ws + FLAG_OFF + ((halo && lane >= 16) ? prev : own) * 256);
    for (unsigned n = 0; n < (1u << 16); ++n) {
      u64 v = E + ev;
      if (lane < (halo ? 32 : 16)) v = __hip_atomic_load(f, RLX_AGENT);
      if (__all(v - (E + ev) <= (u64)(10u - ev))) break;
    }
    __builtin_amdgcn_fence(__ATOMIC_ACQUIRE, "wavefront");
    sig<SY_READY>(vs, ev);
  };
  Tail tl;
  sfor<0, 2>([&](auto PASS) {
    constexpr int pass = PASS.v;
    hop(1 + 5 * pass, true);
    STAMP(1 + 4 * pass);
    {
      const int hb = t > 0 ? 4 * t - 1 : 0, G = lane >> 4;
      const int hvo = (G >> 1) * 1024 + (16 + (lane & 15) + 32 * (G & 1)) * 16, xb = X_OFF + 2 * pass * X_SZ + hb * 32768;
      v4u a[4], hx[16];
#pragma unroll
      for (int i = 0; i < 4; ++i) a[i] = ldx(rs, lane * 16 + i * 1024, ST_OFF + 2 * pass * ST_SZ + hb * 4096);
#pragma unroll
      for (int i = 0; i < 16; ++i) hx[i] = ldx(rs, hvo + (i & 1) * 2048, xb + (i >> 1) * 4096);
      SBAR();
      const v4u r = ln_stats(a);
      if (lane >= 8 && lane < 16) *(LAS v4u*)(L + LN_OFF + (2 * lane - 16) * 8) = r;
#pragma unroll
      for (int i = 0; i < ((AB & 4) ? 1 : 16); ++i) *(LAS v4u*)(L + EX_OFF + i * 1024 + lane * 16) = hx[i];
      sig<SY_LN>(vs, pass + 1);
    }
#pragma nounroll
    for (unsigned e = 2; e <= 5; ++e) {
      hop(e + 5 * pass, false);
      if (e != 5) STAMP(e + 4 * pass); else STAMP(62 + pass);
      if (pass == 0 && e == 3) tl.load(rs, vs, lane, j, 0);
    }
    tl.put(L, vs, lane, 0, pass);
  });
  if (blockIdx.x == 0) {
    for (unsigned n = 0; n < (1u << 16); ++n) {
      bool ok = true;
#pragma unroll
      for (int i = 0; i < 4; ++i) ok &= (__hip_atomic_load((GAS u64*)(p.ws + FLAG_OFF + (i * 64 + lane) * 256), RLX_AGENT) - (E + 3) <= 7ull);
      if (__all(ok)) break;
    }
    if (lane == 0) __hip_atomic_store((GAS u64*)(p.ws + EPOCH_OFF), E + 16, RLX_AGENT);
  }
}

template <int S> DI v4u ldf(__amdgpu_buffer_rsrc_t rs, int voff, int base) { return ldx(rs, voff + (S & 3) * 1024, base + (S >> 2) * 4096); }
struct LB { LAS char* b[3]; DI explicit LB(LAS char* x) { b[0] = opqv(x); b[1] = opqv(x + 60 * 1024); b[2] = opqv(x + 120 * 1024); }
  template <int OFF> DI LAS char* at() const { return b[OFF / (60 * 1024)] + OFF % (60 * 1024); } };

#define HOLDMASK 0x01
template <int PH> DI void hold(unsigned vs, unsigned ev) { if constexpr ((HOLDMASK >> PH) & 1) sig<SY_HOLD>(vs, ev); }
struct Resid { v4u a, b; };
DI Resid ld_resid(__amdgpu_buffer_rsrc_t rs, int lane, int wave, int t, int j, int r_off) {
  const int ro = opq(r_off + ((4 * t + 2 * (j & 1) + (wave & 1)) * 32 + 4 * (j >> 1) + 2 * (wave >> 1)) * 1024);
  return Resid{ldf<0>(rs, lane * 16, ro), ldf<1>(rs, lane * 16, ro)};
}
template <int NKS, bool IS_DOWN>
DI void proj_phase(const Params& p, LAS char* L, unsigned vs, __amdgpu_buffer_rsrc_t rs, int lane, int wave, int t, int j, int pass, unsigned& have,
                   int a_off  , const Resid& res, int x_off, int st_off, unsigned ev  , unsigned ph) {
  const int m2 = wave & 1, kh = wave >> 1, th = j & 1, jc = j >> 1, tb = 4 * t + 2 * th + m2;
  const int h = lane >> 5;
  const unsigned pb = pass * P_END, vsw = vs + 4 * wave;
  const unsigned vq = lane == 0 ? vsw + (SY_CONS - SY_OFF) : vs + (SY_DUMMY - SY_OFF) + 4 * lane;
  f32x16 acc[2];
#pragma unroll
  for (int i = 0; i < 4; ++i) { acc[0][2 * i] = bflo(res.a[i]); acc[0][2 * i + 1] = bfhi(res.a[i]); acc[0][8 + 2 * i] = bflo(res.b[i]); acc[0][9 + 2 * i] = bfhi(res.b[i]); }
#pragma unroll
  for (int i = 0; i < 16; ++i) acc[1][i] = 0.f;
  asm volatile("" : "+v"(acc[0]));
  auto posk = [](int a) { return IS_DOWN ? pos4(a) : pos2(a); };
  const LB b0(L + lane * 16 + kh * 3072), b1(L + lane * 16 + kh * 1024 + 1024);
  auto rd = [&](auto N) -> s16x8 {
    constexpr int a = N.v >> 1, c = N.v & 1, sl = slot_of(IS_DOWN ? pos4(a) : pos2(a));
    const LB& b = c ? b1 : b0;
    return *(LAS s16x8*)b.template at<sl>();
  };
  constexpr int NOP = 2 * NKS, PF = PFD, LAST = IS_DOWN ? T4 : NKS;
  s16x8 wf[PF];
  wait_landed(vs, have, pb + posk(8));
  sfor<0, PF>([&](auto N) { wf[N.v] = rd(N); });
  const int ab = opq(a_off + tb * (IS_DOWN ? 86 : 32) * 1024 + (IS_DOWN ? 0 : kh * 16384)), khk = opq(kh * 1024);
  v4u xf[NKS];
  auto ldk = [&](auto K) {
    constexpr int u0 = ord4(0, K.v), u1 = ord4(1, K.v);
    if constexpr (IS_DOWN) xf[K.v] = ldx(rs, lane * 16, ab + u0 * 1024 + khk * (u1 - u0));
    else xf[K.v] = ldf<K.v>(rs, lane * 16, ab);
  };
  SBAR();
  spin_ge<SY_READY>(vs, ev + (IS_DOWN && LATE0 == 16 ? 1 : 0));
  STAMP(16 + 4 * ph);
  CDECL(); if (IS_DOWN) CST(4, 0);
  sfor<0, XW>([&](auto S) { ldk(S); });
  SBAR();
  v2u pl; unsigned pf;
  sfor<0, NOP>([&](auto N) {
    constexpr int n = N.v;
    const s16x8 cur = wf[n % PF];
    if constexpr (n + PF < NOP) {
      constexpr int a2 = (n + PF) >> 1;
      if constexpr (IS_DOWN && n + PF == 18) sigq(vq, pb + posk(9));
      if constexpr (((n + PF) & 15) == 10 && a2 + 3 < LAST) pl = peek_landed(vs);
      if constexpr (((n + PF) & 15) == 15 && a2 + 1 < LAST) take_landed(have, pl);
      if constexpr (((n + PF) & 15) == 0 && a2 < LAST) wait_landed(vs, have, pb + posk(a2 + 8 < LAST ? a2 + 8 : LAST));
      if constexpr (IS_DOWN && n + PF == 2 * T4 - 5) pf = peek(vs + 4 * (lane & 1), SY_T4);
      if constexpr (IS_DOWN && n + PF == 2 * T4 - 1) need_all<SY_T4>(pf, vs + 4 * (lane & 1), pass + 1);
      wf[n % PF] = rd(IC<n + PF>{});
    }
    acc[n & 1] = mfma32(cur, xf[n >> 1], acc[n & 1]);
    if constexpr (IS_DOWN && n == LATE0 - 5) pf = peek(vs, SY_READY);
    if constexpr (IS_DOWN && n == LATE0 - 1) need_all<SY_READY>(pf, vs, ev + 1);
    constexpr int k = XW + n < 24 ? XW + n : (n >= LATE0 ? 24 + n - LATE0 : NKS);
    if constexpr (k < NKS) ldk(IC<k>{});
    if constexpr (IS_DOWN) { if constexpr (n == 1) CST(4, 1); if constexpr (n == 15) CST(4, 2); if constexpr (n == 31) CST(4, 3); if constexpr (n == 47) CST(4, 4); if constexpr (n == 63) CST(4, 5); if constexpr (n == 75) CST(4, 6); if constexpr (n == 85) CST(4, 7); }
    SBAR();
  });
  asm volatile("" : "+v"(acc[0]), "+v"(acc[1]));
  sigq(vq, pb + (IS_DOWN ? P_END : posk(NKS)));
  const unsigned evo = ev + (IS_DOWN ? 2 : 1);
  if (!(IS_DOWN && pass == 1)) hold<IS_DOWN ? 4 : 2>(vs, evo);
  if (IS_DOWN) CDUMP(4);
  STAMP(17 + 4 * ph);
#pragma unroll
  for (int g = 0; g < 4; ++g) *(LAS f32x4*)(L + EX_OFF + wave * 4096 + g * 1024 + lane * 16) = f32x4{acc[1][4 * g], acc[1][4 * g + 1], acc[1][4 * g + 2], acc[1][4 * g + 3]};
  sig<SY_XF>(vsw, evo);
  spin_ge<SY_XF>(vs + 4 * (wave ^ 2), evo);
  unsigned o[8]; f32x2 xv[8];
#pragma unroll
  for (int g = 0; g < 4; ++g) {
    const f32x4 e = *(LAS f32x4*)(L + EX_OFF + (wave ^ 2) * 4096 + g * 1024 + lane * 16);
    xv[2 * g] = f32x2{acc[0][4 * g], acc[0][4 * g + 1]} + f32x2{e[0], e[1]};
    xv[2 * g + 1] = f32x2{acc[0][4 * g + 2], acc[0][4 * g + 3]} + f32x2{e[2], e[3]};
    o[2 * g] = pk(xv[2 * g]); o[2 * g + 1] = pk(xv[2 * g + 1]);
  }
  if (IS_DOWN && pass == 1) {
    GAS char* dst = (GAS char*)p.out + (size_t)(tb * 32 + (lane & 31)) * 1024 + (64 * jc + 32 * kh + 4 * h) * 2;
#pragma unroll
    for (int g = 0; g < 4; ++g) *(GAS v2u*)(dst + 16 * g) = v2u{o[2 * g], o[2 * g + 1]};
    STAMP(18 + 4 * ph);
    return;
  }
  const int xo = x_off + (tb * 32 + 4 * jc + 2 * kh) * 1024 + lane * 16;
  stx(v4u{o[0], o[1], o[2], o[3]}, rs, xo);
  stx(v4u{o[4], o[5], o[6], o[7]}, rs, xo + 1024);
  const f32x2 s2 = ((xv[0] + xv[1]) + (xv[2] + xv[3])) + ((xv[4] + xv[5]) + (xv[6] + xv[7]));
  const float sum = sum32(s2.x + s2.y);
  const f32x2 mi = sp(sum * (-1.f / 32.f));
  f32x2 q0 = {0.f, 0.f}, q1 = {0.f, 0.f};
#pragma unroll
  for (int i = 0; i < 4; ++i) { const f32x2 d0 = xv[2 * i] + mi, d1 = xv[2 * i + 1] + mi; q0 = fma2(d0, d0, q0); q1 = fma2(d1, d1, q1); }
  q0 += q1;
  const float m2s = sum32(q0.x + q0.y);
  if (lane < 32) stx2(v2u{asu(sum), asu(m2s)}, rs, lane * 8 + st_off + (tb * 16 + 2 * jc + kh) * 256);
  STAMP(18 + 4 * ph);
  publish(vsw, evo);
  STAMP(19 + 4 * ph);
}

DI void compute_main(const Params& p, LAS char* L, unsigned vs, int lane, int wave, int t, int j) {
  const __amdgpu_buffer_rsrc_t rs = __builtin_amdgcn_make_buffer_rsrc((void*)p.ws, 0, WS_BYTES, 0x00020000);
  unsigned have = 0;
  const unsigned vsw = vs + 4 * wave;
  STAMP(10);
  {
    const int c = lane >> 3, tk = lane & 7, odd = c & 1, blk = 4 * t + (j >> 2), sl = 8 * (j & 3) + tk;
    const GAS char* src = (const GAS char*)p.x + (size_t)(128 * t + 8 * j + tk) * 1024 + (16 * wave + c) * 16;
    v4u d[2];
    d[0] = *(const GAS v4u*)src; d[1] = *(const GAS v4u*)(src + 128);
    SBAR();
    STAMP(14);
#pragma unroll
    for (int a = 0; a < 2; ++a) {
      const int cI = 16 * wave + 8 * a + c;
      const unsigned r0 = ror8(odd ? d[a].x : d[a].z), r1 = ror8(odd ? d[a].y : d[a].w);
      const v4u o = odd ? v4u{r0, r1, d[a].z, d[a].w} : v4u{d[a].x, d[a].y, r0, r1};
      stx(o, rs, (cI >> 1) * 1024 + (sl + 32 * odd) * 16 + X_OFF + blk * 32768);
      float xv[8], s = 0.f;
#pragma unroll
      for (int i = 0; i < 4; ++i) { xv[2 * i] = bflo(d[a][i]); xv[2 * i + 1] = bfhi(d[a][i]); s += xv[2 * i] + xv[2 * i + 1]; }
      s += asf(ror8(asu(s))); s = sum16(s);
      const float mi = s * (1.f / 32.f); float m2 = 0.f;
#pragma unroll
      for (int i = 0; i < 8; ++i) { const float e = xv[i] - mi; m2 += e * e; }
      m2 += asf(ror8(asu(m2))); m2 = sum16(m2);
      if ((c & 3) == 0) stx2(v2u{asu(s), asu(m2)}, rs, (cI >> 2) * 256 + sl * 8 + ST_OFF + blk * 4096);
    }
    hold<0>(vs, 1);
    STAMP(11);
    publish(vsw, 1);
    STAMP(12);
    {
      const int us = (j / 3) * 16 + (j % 3) * 6, up = wave & 1;
      const v4u r3 = srd(up ? p.wu : p.wg, 512 * 2752);
#pragma unroll
      for (int q = 0; q < 3; ++q) {
        const int e = 64 * q + lane, r = (e * 43691) >> 19, c = e - 12 * r;
        dma1((unsigned)(size_t)L + STG3 + wave * 3072 + q * 1024, r * 2752 + (c ^ (2 * up)) * 16 + 32 * us, r3, (2 * t + (wave >> 1)) * (16 * 2752));
      }
      DRAIN(); sig<SY_RAW>(vsw, 1u);
    }
  }
#pragma nounroll
  for (int pass = 0; pass < 2; ++pass) {
    const unsigned pb = pass * P_END, ph = 4 * pass + 1, eb = 5 * pass + 1;
    Resid res;
    const int xin = X_OFF + 2 * pass * X_SZ, stin = ST_OFF + 2 * pass * ST_SZ;
    const int tb = 4 * t + wave;
    {
      int ln = lane; asm volatile("" : "+v"(ln));
      const int h = ln >> 5, tok = ln & 31;
      CDECL();
      v4u xf[32], hx[16]; LnSteps lns;
      const int xb = opq(xin + tb * 32768), sb = opq(stin + tb * 4096);
      const int G = ln >> 4;
      const LB b1(L + ln * 16);
      f32x16 acc[3];
#pragma unroll
      for (int c = 0; c < 3; ++c)
#pragma unroll
        for (int i = 0; i < 16; ++i) acc[c][i] = 0.f;
      constexpr int PF = PFD;
      s16x8 wf[PF];
      const int oh = (16 * (wave >> 1) + (ln & 15) + 32 * (G & 1)) * 16 + (wave & 1) * 1024;
      const LB bh(L + oh + (G >> 1) * 3072);
      LAS char* bh2 = opqv(L + oh + (G >> 1) * 2048 + 3 * QA * 1024);
      auto rd = [&](auto N) -> s16x8 {
        constexpr int n = N.v;
        if constexpr (n >= 88 && n < 88 + QA / 2) return *(LAS s16x8*)bh.at<(n - 88) * 6144>();
        else if constexpr (n >= 88 && n < 104) return *(LAS s16x8*)(bh2 + (n - 88 - QA / 2) * 4096);
        else { constexpr int o = (n < 88 ? n : n - 16) * 1024; return *(LAS s16x8*)b1.at<o>(); }
      };
      f32x4 hacc = {0.f, 0.f, 0.f, 0.f};
      const unsigned vq = ln == 0 ? vsw + (SY_CONS - SY_OFF) : vs + (SY_DUMMY - SY_OFF) + 4 * ln, vs4 = vs + 4 * (ln & 3);
      LAS char* csb = opqv(L + CS1_OFF + 16 * h);
      LAS char* vw = opqv(L + EX_OFF + 2 * h * PLANE + (16 + 32 * wave + tok) * 16);
      LAS char* kw = opqv(L + KP_OFF + h * PLANE + (16 + 32 * wave + tok) * 16);
      LAS char* hxb = opqv(L + EX_OFF + ln * 16);
      wait_landed(vs, have, pb + 24);
      sfor<0, PF>([&](auto N) { wf[N.v] = rd(N); });
      SBAR();
      spin_ge<SY_READY>(vs, eb);
      STAMP(16 + 4 * ph);
      CST(5, 0);
      v2u pl; unsigned pf, pc; f32x4 cs[12]; f32x4 csh; f32x2 lnh;
      sfor<0, XW>([&](auto S) { xf[S.v] = ldf<S.v>(rs, ln * 16, xb); });
      SBAR();
      CST(5, 1);
      auto op = [&](auto N) {
        constexpr int n = N.v, r = n + PF;
        const s16x8 cur = wf[n % PF];
        if constexpr (r == 104) sigq(vq, pb + KP_OFF / 1024);
        if constexpr (r < 112) wf[n % PF] = rd(IC<r>{});
        constexpr int st = n < 3 * QA ? n / 3 : QA + (n - 3 * QA) / 2, c = n < 3 * QA ? (n % 3 + 1) % 3 : 1 + (n - 3 * QA) % 2;
        if constexpr (n < 88) acc[c] = mfma32(cur, xf[st], acc[c]);
        else if constexpr (n < 104) hacc = mfma16(cur, hx[n - 88], hacc);
        else acc[0] = mfma32(cur, xf[n - 80], acc[0]);
      };
      sfor<0, 104>([&](auto N) {
        constexpr int n = N.v, r = n + PF;
        if constexpr (r % 24 == 17 && r < 72) pl = peek_landed(vs);
        if constexpr (r % 24 == 23 && r < 72) take_landed(have, pl);
        if constexpr (r % 24 == 0 && r < 96) wait_landed(vs, have, pb + r + 24);
        op(N);
        {
          constexpr int k = n % 3 == 2 ? 32 : XW + 2 * (n / 3) + n % 3;
          if constexpr (k < 32) xf[k] = ldf<k>(rs, ln * 16, xb);
          if constexpr (n % 3 == 2 && n / 3 >= 4 && n / 3 < 12) lns.st[n / 3 - 4] = ldx2(rs, h * 2048 + tok * 8 + (n / 3 - 4) * 256, sb);
        }
        if constexpr (n >= 50 && n < 50 + LnSteps::N) lns.template step<n - 50>();
        if constexpr (n == 74) { pf = peek(vs, SY_LN); pc = peek(vs, SY_CS); }
        if constexpr (n == 79) need_all<SY_LN>(pf, vs, pass + 1);
        if constexpr (n == 81) need_all<SY_CS>(pc, vs, 1u);
        if constexpr (n >= 84 && n < 100) hx[n - 84] = *(LAS v4u*)(hxb + (n - 84) * 1024);
        if constexpr (n >= 100 && n < 104) cs[n - 100] = *(LAS f32x4*)(csb + (32 + 8 * (n - 100)) * 4);
        if constexpr (n == 2) CST(5, 2); if constexpr (n == 23) CST(5, 3); if constexpr (n == 47) CST(5, 4); if constexpr (n == 71) CST(5, 5); if constexpr (n == 87) CST(5, 6); if constexpr (n == 103) CST(5, 7);
        SBAR();
      });
      asm volatile("" : "+v"(acc[1]), "+v"(acc[2]), "+v"(hacc));
      STAMP(17 + 4 * ph);
      CST(1, 0);
      const f32x2 rstd = sp(lns.rstd), nmean = sp(lns.nmean);
      {
        unsigned pk_[8]; f32x2 vv[8];
        op(IC<104>{});
#pragma unroll
        for (int i = 4; i < 8; ++i) cs[i] = *(LAS f32x4*)(csb + (32 + 8 * i) * 4);
        csh = *(LAS f32x4*)(L + CS1_OFF + (32 * (1 + (wave & 1)) + 16 * (wave >> 1) + 4 * G) * 4); lnh = *(LAS f32x2*)(L + LN_OFF + (ln & 15) * 8);
        SBAR(); op(IC<105>{}); SBAR();
        sfor<0, 8>([&](auto I) { constexpr int i = I.v; pk_[i] = pk(fma2(f32x2{cs[i >> 1][2 * (i & 1)], cs[i >> 1][2 * (i & 1) + 1]}, nmean, pr<i>(acc[1])) * rstd); });
        pf = peek(vs4, SY_CONS);
        SBAR(); op(IC<106>{}); op(IC<107>{}); SBAR();
        sfor<0, 8>([&](auto I) { constexpr int i = I.v; vv[i] = fma2(f32x2{cs[4 + (i >> 1)][2 * (i & 1)], cs[4 + (i >> 1)][2 * (i & 1) + 1]}, nmean, pr<i>(acc[2])) * rstd; });
        const f32x2 hr = sp(t == 0 ? 0.f : lnh.x), hm = sp(t == 0 ? 0.f : lnh.y);
        f32x2 hv0 = fma2(f32x2{csh[0], csh[1]}, hm, f32x2{hacc[0], hacc[1]}) * hr, hv1 = fma2(f32x2{csh[2], csh[3]}, hm, f32x2{hacc[2], hacc[3]}) * hr;
        if (t == 0) { hv0 = sp(0.f); hv1 = sp(0.f); }
        SBAR();
        CST(1, 1);
        need_all<SY_CONS>(pf, vs4, pb + KP_OFF / 1024);
        op(IC<108>{});
        *(LAS v4u*)kw = v4u{pk_[0], pk_[1], pk_[2], pk_[3]};
        *(LAS v4u*)(kw + 2 * PLANE) = v4u{pk_[4], pk_[5], pk_[6], pk_[7]};
        SBAR(); op(IC<109>{}); SBAR();
        if (wave & 1) *(LAS f32x4*)(L + EX_OFF + ((wave >> 1) * 4 + 2 * (G & 1) + (G >> 1)) * PLANE + (ln & 15) * 16) = f32x4{hv0.x, hv0.y, hv1.x, hv1.y};
        else *(LAS v2u*)(L + KP_OFF + ((wave >> 1) * 2 + (G & 1)) * PLANE + (ln & 15) * 16 + 8 * (G >> 1)) = v2u{pk(hv0), pk(hv1)};
        sig<SY_KV>(vsw, 2 * pass + 1);
        SBAR(); op(IC<110>{}); SBAR();
#pragma unroll
        for (int g = 0; g < 4; ++g) *(LAS f32x4*)(vw + ((g >> 1) * 4 + (g & 1)) * PLANE) = f32x4{vv[2 * g].x, vv[2 * g].y, vv[2 * g + 1].x, vv[2 * g + 1].y};
        sig<SY_KV>(vsw, 2 * pass + 2);
        SBAR(); op(IC<111>{});
#pragma unroll
        for (int i = 8; i < 12; ++i) cs[i] = *(LAS f32x4*)(csb + 8 * (i - 8) * 4);
        res = ld_resid(rs, ln, wave, t, j, xin);
        SBAR();
      }
      CST(1, 2);
      spin_all_ge<SY_KV>(vs4, 2 * pass + 1);
      CST(1, 3);
      LAS char* kb = L + KP_OFF + h * 2 * PLANE + (16 + 32 * wave + tok - 14) * 16;
      LAS char* vb = L + EX_OFF + h * 4 * PLANE + (16 + 32 * wave + tok - 14) * 16;
      float lg[8]; v4u kk[16]; f32x4 vr[32];
#pragma unroll
      for (int w = 0; w < 8; ++w) { kk[2 * w] = *(LAS v4u*)(kb + (14 - 2 * w) * 16); kk[2 * w + 1] = *(LAS v4u*)(kb + PLANE + (14 - 2 * w) * 16); }
      SBAR();
      unsigned qq[8];
      {
        unsigned pq[8];
        sfor<0, 8>([&](auto I) { constexpr int i = I.v; pq[i] = pk(fma2(f32x2{cs[8 + (i >> 1)][2 * (i & 1)], cs[8 + (i >> 1)][2 * (i & 1) + 1]}, nmean, pr<i>(acc[0]))); });
#pragma unroll
        for (int i = 0; i < 4; ++i) { auto r = __builtin_amdgcn_permlane32_swap(pq[i], pq[4 + i], false, false); qq[i] = r[0]; qq[4 + i] = r[1]; }
      }
      pf = peek(vs4, SY_KV);
      SBAR();
      CST(1, 4);
      need_all<SY_KV>(pf, vs4, 2 * pass + 2);
      sfor<0, 4>([&](auto W) {
        constexpr int w = 2 * W.v;
#pragma unroll
        for (int g = 0; g < 8; ++g) vr[4 * w + g] = *(LAS f32x4*)(vb + (g & 3) * PLANE + (14 - 2 * (w + (g >> 2))) * 16);
        float a = dot2z(qq[0], kk[2 * w].x), b = dot2z(qq[0], kk[2 * w + 2].x);
#pragma unroll
        for (int i = 1; i < 8; ++i) { a = dot2(qq[i], kk[2 * w + (i >> 2)][i & 3], a); b = dot2(qq[i], kk[2 * w + 2 + (i >> 2)][i & 3], b); }
        lg[w] = a; lg[w + 1] = b;
        SBAR();
      });
      asm volatile("" : "+v"(lg[0]), "+v"(lg[1]), "+v"(lg[2]), "+v"(lg[3]), "+v"(lg[4]), "+v"(lg[5]), "+v"(lg[6]), "+v"(lg[7]));
      sigq(vq, pb + P_W2);
      hold<1>(vs, eb + 1);
      CST(1, 6);
      if (t == 0 && wave == 0) {
#pragma unroll
        for (int w = 1; w < 8; ++w) lg[w] = tok >= 2 * w ? lg[w] : -1e30f;
      }
      const float mx = max3(max3(lg[0], lg[1], lg[2]), max3(lg[3], lg[4], lg[5]), max3(lg[6], lg[7], lg[7]));
      const float sc = lns.rstd * (0.25f * 1.4426950408889634f);
      f32x2 e2[4];
#pragma unroll
      for (int w = 0; w < 4; ++w) { const f32x2 z = (f32x2{lg[2 * w], lg[2 * w + 1]} - sp(mx)) * sp(sc); e2[w] = f32x2{__builtin_amdgcn_exp2f(z.x), __builtin_amdgcn_exp2f(z.y)}; }
      const f32x2 s2 = (e2[0] + e2[1]) + (e2[2] + e2[3]);
      const f32x2 inv = sp(__builtin_amdgcn_rcpf(s2.x + s2.y));
      f32x2 ov[8];
#pragma unroll
      for (int w = 0; w < 8; ++w) {
        const f32x2 pw = sp(e2[w >> 1][w & 1]);
#pragma unroll
        for (int g = 0; g < 4; ++g) {
          const f32x4 v = vr[4 * w + g];
          ov[2 * g] = w ? fma2(f32x2{v[0], v[1]}, pw, ov[2 * g]) : f32x2{v[0], v[1]} * pw;
          ov[2 * g + 1] = w ? fma2(f32x2{v[2], v[3]}, pw, ov[2 * g + 1]) : f32x2{v[2], v[3]} * pw;
        }
      }
      const int oo = O_OFF + pass * X_SZ + (tb * 32 + 2 * j) * 1024 + h * 1024 + tok * 16;
      stx(v4u{pk(ov[0] * inv), pk(ov[1] * inv), pk(ov[2] * inv), pk(ov[3] * inv)}, rs, oo);
      stx(v4u{pk(ov[4] * inv), pk(ov[5] * inv), pk(ov[6] * inv), pk(ov[7] * inv)}, rs, oo + 512);
      CST(1, 7); CDUMP(1); CDUMP(5);
      STAMP(18 + 4 * ph);
      publish(vsw, eb + 1);
      STAMP(19 + 4 * ph);
    }
    { int ln = lane; asm volatile("" : "+v"(ln));
      proj_phase<16, false>(p, L, vs, rs, ln, wave, t, j, pass, have, O_OFF + pass * X_SZ, res, xin + X_SZ, stin + ST_SZ, eb + 1, ph + 1); }
    {
      int ln = lane; asm volatile("" : "+v"(ln));
      const int h = ln >> 5, tok = ln & 31;
      CDECL();
      v4u xf[32]; LnSteps lns;
      const int xb = opq(xin + X_SZ + tb * 32768), sb = opq(stin + ST_SZ + tb * 4096);
      const LB b3(L + ln * 16);
      f32x16 acc[6];
#pragma unroll
      for (int c = 0; c < 6; ++c)
#pragma unroll
        for (int i = 0; i < 16; ++i) acc[c][i] = 0.f;
      constexpr int PF = PFD;
      s16x8 wf[PF];
      auto rd = [&](auto N) -> s16x8 { return *(LAS s16x8*)b3.template at<slot_of(P_W3 + N.v)>(); };
      const unsigned vq = ln == 0 ? vsw + (SY_CONS - SY_OFF) : vs + (SY_DUMMY - SY_OFF) + 4 * ln;
      wait_landed(vs, have, pb + pos3(4));
      sfor<0, PF>([&](auto N) { wf[N.v] = rd(N); });
      SBAR();
      spin_ge<SY_READY>(vs, eb + 2);
      v2u pl; unsigned pc;
      STAMP(16 + 4 * (ph + 2));
      CST(3, 0);
      sfor<0, XW>([&](auto S) { xf[S.v] = ldf<S.v>(rs, ln * 16, xb); });
      SBAR();
      sfor<0, 192>([&](auto N) {
        constexpr int n = N.v;
        const s16x8 cur = wf[n % PF];
        if constexpr (n + PF < 192) {
          if constexpr (((n + PF) % 12 == 2 && n + PF > 12) || (n + PF) == 188) sigq(vq, pb + pos3((n + PF) / 6));
          if constexpr (n == 100) pc = peek(vs, SY_CS);
          if constexpr (n == 106) need_all<SY_CS>(pc, vs, 2u);
          if constexpr (!(AB & 8) && (n + PF) % 24 == 14 && n + PF < 24 * 7) pl = peek_landed(vs);
          if constexpr (!(AB & 8) && (n + PF) % 24 == 20 && n + PF < 24 * 7) take_landed(have, pl);
          if constexpr (!(AB & 8) && (n + PF) % 24 == 22 && n + PF < 24 * 7) wait_landed(vs, have, pb + pos3((n + PF) / 6 + 5));
          wf[n % PF] = rd(IC<n + PF>{});
        }
        acc[n % 6] = mfma32(cur, xf[n / 6], acc[n % 6]);
        {
          constexpr int st_ = n / 6, k = n % 6 == 0 ? XW + st_ + (st_ < E3 ? st_ : E3) : (n % 6 == 4 && st_ < E3 ? XW + 2 * st_ + 1 : 32);
          if constexpr (k < 32) { if constexpr (AB & 1) xf[k] = xf[k % 8]; else xf[k] = ldf<k>(rs, ln * 16, xb); }
        }
        if constexpr (!(AB & 2) && n % 6 == 2 && n / 6 < 8) lns.st[n / 6] = ldx2(rs, h * 2048 + tok * 8 + (n / 6) * 256, sb);
        if constexpr (!(AB & 2) && n >= 90 && n % 3 == 0 && (n - 90) / 3 < LnSteps::N) lns.template step<(n - 90) / 3>();
        if constexpr (n == 5) CST(3, 1); if constexpr (n == 47) CST(3, 2); if constexpr (n == 95) CST(3, 3); if constexpr (n == 119) CST(3, 4); if constexpr (n == 143) CST(3, 5); if constexpr (n == 167) CST(3, 6); if constexpr (n == 191) CST(3, 7);
        SBAR();
      });
      asm volatile("" : "+v"(acc[0]), "+v"(acc[1]), "+v"(acc[2]), "+v"(acc[3]), "+v"(acc[4]), "+v"(acc[5]));
      sig<SY_CONS>(vsw, pb + P_W4);
      CDUMP(3);
      STAMP(17 + 4 * (ph + 2));
      const f32x2 k1 = sp(lns.rstd * -2.3022081985f), k3 = sp(lns.rstd * lns.rstd * lns.rstd * -0.1029432397f), sd2 = sp(lns.sd * lns.sd), nmean = sp(lns.nmean);
      const int us = (j / 3) * 16 + (j % 3) * 6, nu = (j % 3 == 2) ? 4 : 6;
      const int ho = H_OFF + pass * H_SZ + (tb * 86 + us) * 1024 + ln * 16;
#pragma unroll
      for (int u = 0; u < 6; ++u) {
        if (u < 4 || nu == 6) {
          unsigned hv[4];
#pragma unroll
          for (int g = 0; g < 2; ++g) {
            const f32x4 cg = *(LAS f32x4*)(L + CS3_OFF + (32 * u + 8 * g + 4 * h) * 4), cu = *(LAS f32x4*)(L + CS3_OFF + (32 * u + 16 + 8 * g + 4 * h) * 4);
#pragma unroll
            for (int i = 0; i < 2; ++i) {
              const f32x2 tg = fma2(f32x2{cg[2 * i], cg[2 * i + 1]}, nmean, f32x2{acc[u][4 * g + 2 * i], acc[u][4 * g + 2 * i + 1]});
              const f32x2 tu = fma2(f32x2{cu[2 * i], cu[2 * i + 1]}, nmean, f32x2{acc[u][8 + 4 * g + 2 * i], acc[u][8 + 4 * g + 2 * i + 1]});
              const f32x2 z = tg * fma2(tg * tg, k3, k1);
              const f32x2 d = fma2(f32x2{__builtin_amdgcn_exp2f(z.x), __builtin_amdgcn_exp2f(z.y)}, sd2, sd2);
              hv[2 * g + i] = pk(tg * tu * f32x2{__builtin_amdgcn_rcpf(d.x), __builtin_amdgcn_rcpf(d.y)});
            }
          }
          stx(v4u{hv[0], hv[1], hv[2], hv[3]}, rs, ho + u * 1024);
          if (u == 3) { asm volatile("s_waitcnt vmcnt(1)" ::: "memory"); sig<SY_PROG>(vsw, eb + 3); }
        }
        SBAR();
      }
      res = ld_resid(rs, ln, wave, t, j, xin + X_SZ);
      STAMP(18 + 4 * (ph + 2));
      hold<3>(vs, eb + 4);
      publish(vsw, eb + 4);
      STAMP(19 + 4 * (ph + 2));
    }
    { int ln = lane; asm volatile("" : "+v"(ln));
      proj_phase<43, true>(p, L, vs, rs, ln, wave, t, j, pass, have, H_OFF + pass * H_SZ, res, xin + 2 * X_SZ, stin + 2 * ST_SZ, eb + 3, ph + 3); }
  }
}

__global__ void __launch_bounds__(512, 2) fused_kernel(Params p) {
  extern __shared__ __attribute__((aligned(16))) char smem[];
  LAS char* L = (LAS char*)smem;
  const int lane = threadIdx.x & 63, wave = __builtin_amdgcn_readfirstlane(threadIdx.x >> 6);
  const int b = blockIdx.x;
  const int t = (b & 7) * 2 + (b >> 7), j = (b >> 3) & 15;
  asm volatile(".subsection 2\n\t.fill 128, 4, 0xBF800000\n\t.subsection 0");
  if (threadIdx.x < 40) *(LAS unsigned*)(L + SY_OFF + threadIdx.x * 4) = 0u;
  __syncthreads();
  const unsigned lb = (unsigned)(size_t)L;
  unsigned vs = lb + SY_OFF; asm volatile("" : "+v"(vs));
  if (wave < 4) { __builtin_amdgcn_s_setprio(2); compute_main(p, L, vs, lane, wave, t, j); }
  else if (wave == 4) { __builtin_amdgcn_s_setprio(3); poller_main(p, L, vs, lane, t, j, wave); }
  else if (wave == 5) { __builtin_amdgcn_s_setprio(3); helper_main(p, L, lb, vs, lane, t, j); }
  else {
    loader_main(p, L, lb, lane, t, j, wave - 6, wave); }
  PROF_DUMP();
}
}

extern "C" void kernel_launch(void* const* d_in, const int* in_sizes, int n_in, void* d_out, int out_size, void* d_ws, size_t ws_size, hipStream_t stream) {
  Params p;
  p.x = (const u16*)d_in[0]; p.wqkv = (const u16*)d_in[1]; p.wo = (const u16*)d_in[2];
  p.wg = (const u16*)d_in[4]; p.wu = (const u16*)d_in[5]; p.wd = (const u16*)d_in[6];
  p.out = (u16*)d_out; p.ws = (unsigned char*)d_ws;
  (void)hipFuncSetAttribute((const void*)fused_kernel, hipFuncAttributeMaxDynamicSharedMemorySize, LDS_ALLOC);
  hipLaunchKernelGGL(fused_kernel, dim3(256), dim3(512), LDS_ALLOC, stream, p);
}
```

```cpp
#include <hip/hip_runtime.h>
#include <hip/hip_bf16.h>

#define GAS __attribute__((address_space(1)))
#define LAS __attribute__((address_space(3)))
#define DI __device__ __forceinline__
typedef unsigned v4u __attribute__((ext_vector_type(4)));
typedef unsigned v2u __attribute__((ext_vector_type(2)));
typedef float f32x2 __attribute__((ext_vector_type(2)));
typedef float f32x4 __attribute__((ext_vector_type(4)));
typedef float f32x16 __attribute__((ext_vector_type(16)));
typedef short s16x4 __attribute__((ext_vector_type(4)));
typedef short s16x8 __attribute__((ext_vector_type(8)));
typedef __bf16 bf16x8 __attribute__((ext_vector_type(8)));
typedef __bf16 bf16x2 __attribute__((ext_vector_type(2)));
typedef unsigned long long u64;
typedef unsigned short u16;
#define RLX_AGENT __ATOMIC_RELAXED, __HIP_MEMORY_SCOPE_AGENT

namespace {
constexpr int AUX_SC1 = 16;
constexpr int FLAG_OFF = 0;
constexpr int EPOCH_OFF = 65536;
constexpr int CSF_OFF = 131072;
constexpr int CSP_OFF = 262144;
constexpr int X_OFF = 1 << 20, X_SZ = 2 << 20;
constexpr int O_OFF = X_OFF + 4 * X_SZ;
constexpr int ST_OFF = O_OFF + 2 * X_SZ, ST_SZ = 64 * 16 * 256;
constexpr int H_OFF = ST_OFF + 4 * ST_SZ, H_SZ = 64 * 86 * 1024;
constexpr int WT1_OFF = H_OFF + 2 * H_SZ, WT3_OFF = WT1_OFF + 16 * 96 * 1024, WT2_OFF = WT3_OFF + 16 * 192 * 1024, WT4_OFF = WT2_OFF + 8 * 64 * 1024;
constexpr int WS_BYTES = WT4_OFF + 8 * 172 * 1024;
constexpr int PROF_OFF = 32 << 20;
static_assert(WS_BYTES <= PROF_OFF, "");
constexpr int NR = 136;
constexpr int EX_OFF = NR * 1024, PLANE = 144 * 16;
constexpr int CS1_OFF = EX_OFF + 8 * PLANE, CS3_OFF = CS1_OFF + 96 * 4;
constexpr int LN_OFF = CS3_OFF + 192 * 4;
constexpr int KP_OFF = 79 * 1024;
constexpr int SY_OFF = LN_OFF + 16 * 8;
constexpr int SY_LANDED = SY_OFF;
constexpr int SY_CONS = SY_OFF + 16;
constexpr int SY_WT = SY_OFF + 32, SY_RAW = SY_OFF + 144;
constexpr int SY_HOLD = SY_OFF + 40, SY_READY = SY_OFF + 44;
constexpr int SY_PROG = SY_OFF + 48;
constexpr int SY_KV = SY_OFF + 64;
constexpr int SY_XF = SY_OFF + 80;
constexpr int SY_CS = SY_OFF + 96;
constexpr int SY_T4 = SY_OFF + 104;
constexpr int SY_LN = SY_OFF + 112, SY_T2 = SY_OFF + 116, SY_SR = SY_OFF + 160;
constexpr int SY_TC = SY_OFF + 128;
constexpr int SY_DUMMY = SY_OFF + 176;
constexpr int LDS_BYTES = SY_DUMMY + 256;
constexpr int P_W1 = 0, P_W2 = 96, P_W3 = 160, P_W4 = 352, P_END = 544;
constexpr int QA = 24;
constexpr int pos1(int s, int c  ) { return s < QA ? 3 * s + c : (c < 2 ? 3 * QA + 2 * (s - QA) + c : 64 + s); }
constexpr int pos2(int a) { return P_W2 + 4 * a; }
constexpr int pos3(int s) { return P_W3 + 6 * s; }
constexpr int pos4(int a) { return P_W4 + 4 * a; }
constexpr int T4 = 34;
constexpr int ustart(int j) { return (j / 3) * 16 + (j % 3) * 6; }
constexpr bool early_unit(int kh, int u) { for (int i = 0; i < 8; ++i) if (u >= ustart(8 * kh + i) && u < ustart(8 * kh + i) + 3) return true; return false; }
constexpr int ord4(int kh, int a) {
  if (a < 24) return ustart(8 * kh + a / 3) + a % 3;
  int n = 24;
  for (int u = 43 * kh; u < 43 * kh + 43; ++u) if (!early_unit(kh, u)) { if (n == a) return u; ++n; }
  return -1;
}
static_assert(ord4(0, 42) == 42 && ord4(1, 24) == 43 && ord4(1, 42) == 85, "unit order");
constexpr int slot_of(int p) { return (p % NR) * 1024; }

struct Params { const u16* x; const u16* wqkv; const u16* wo; const u16* wg; const u16* wu; const u16* wd; u16* out; unsigned char* ws; };

DI unsigned rfl(unsigned v) { return __builtin_amdgcn_readfirstlane(v); }
DI u64 uni64(u64 e) { return ((u64)rfl((unsigned)(e >> 32)) << 32) | rfl((unsigned)e); }
DI float asf(unsigned u) { return __builtin_bit_cast(float, u); }
DI unsigned asu(float f) { return __builtin_bit_cast(unsigned, f); }
DI unsigned pk(float a, float b) { bf16x2 v = {(__bf16)a, (__bf16)b}; return __builtin_bit_cast(unsigned, v); }
DI float bflo(unsigned u) { return asf(u << 16); }
DI float bfhi(unsigned u) { return asf(u & 0xffff0000u); }
DI float rbf(float a) { return bflo(pk(a, a)); }
DI float dot2(unsigned a, unsigned b, float c) { return __builtin_amdgcn_fdot2_f32_bf16(__builtin_bit_cast(bf16x2, a), __builtin_bit_cast(bf16x2, b), c, false); }
DI float dot2z(unsigned a, unsigned b) { float r; asm("v_dot2_f32_bf16 %0, %1, %2, 0" : "=v"(r) : "v"(a), "v"(b)); return r; }
DI float max3(float a, float b, float c) { float r; asm("v_max3_f32 %0, %1, %2, %3" : "=v"(r) : "v"(a), "v"(b), "v"(c)); return r; }
DI float add1(float a, float b) { float r; asm("v_add_f32 %0, %1, %2" : "=v"(r) : "v"(a), "v"(b)); return r; }
DI float add3(float a, float b, float c) { return __builtin_constant_p(c) ? add1(a, b) : add1(add1(a, b), c); }
DI f32x2 fma2(f32x2 a, f32x2 b, f32x2 c) { return __builtin_elementwise_fma(a, b, c); }
DI f32x2 sp(float a) { return f32x2{a, a}; }
DI unsigned pk(f32x2 v) { return pk(v.x, v.y); }
template <int I> DI f32x2 pr(const f32x16& v) { return f32x2{v[2 * I], v[2 * I + 1]}; }
DI float sum32(float x) { auto r = __builtin_amdgcn_permlane32_swap(asu(x), asu(x), false, false); return asf(r[0]) + asf(r[1]); }
DI float sum16(float x) { auto r = __builtin_amdgcn_permlane16_swap(asu(x), asu(x), false, false); return asf(r[0]) + asf(r[1]); }
DI unsigned ror8(unsigned x) { return (unsigned)__builtin_amdgcn_update_dpp(0, (int)x, 0x128, 0xf, 0xf, false); }
DI f32x16 mfma32(s16x8 a, v4u b, f32x16 c) { return __builtin_amdgcn_mfma_f32_32x32x16_bf16(__builtin_bit_cast(bf16x8, a), __builtin_bit_cast(bf16x8, b), c, 0, 0, 0); }
DI f32x4 mfma16(s16x8 a, v4u b, f32x4 c) { return __builtin_amdgcn_mfma_f32_16x16x32_bf16(__builtin_bit_cast(bf16x8, a), __builtin_bit_cast(bf16x8, b), c, 0, 0, 0); }
DI s16x4 trr(LAS char* p) { return __builtin_amdgcn_ds_read_tr16_b64_v4i16((LAS s16x4*)p); }
DI s16x8 cat(s16x4 a, s16x4 b) { return __builtin_shufflevector(a, b, 0, 1, 2, 3, 4, 5, 6, 7); }
#define CBAR() asm volatile("" ::: "memory")
#define SBAR() __builtin_amdgcn_sched_barrier(0)
#define DRAIN() asm volatile("s_waitcnt vmcnt(0)" ::: "memory")

#define STAMP(i) do {} while (0)
#define PROF_DUMP() do {} while (0)
constexpr int LDS_ALLOC = LDS_BYTES;

#define CDECL() do {} while (0)
#define CST(site, i) do {} while (0)
#define CDUMP(site) do {} while (0)
template <int I> struct IC { static constexpr int v = I; };
template <int B, int E, class F> DI void sfor(F&& f) { if constexpr (B < E) { f(IC<B>{}); sfor<B + 1, E>(f); } }
DI int opq(int x) { asm volatile("" : "+s"(x)); return x; }
DI LAS char* opqv(LAS char* x) { asm volatile("" : "+v"(x)); return x; }

template <int OFF> DI void spin_ge(unsigned vs, unsigned need) {
  unsigned tv, ts, cnt;
  asm volatile("s_mov_b32 %2, 0x40000\n.Lsp%=:\n\tds_read_b32 %0, %3 offset:%5\n\ts_waitcnt lgkmcnt(0)\n\tv_readfirstlane_b32 %1, %0\n\t"
               "s_cmp_ge_u32 %1, %4\n\ts_cbranch_scc1 .Lsd%=\n\ts_sub_u32 %2, %2, 1\n\ts_cbranch_scc0 .Lsp%=\n.Lsd%=:"
               : "=&v"(tv), "=&s"(ts), "=&s"(cnt) : "v"(vs), "s"(need), "i"(OFF - SY_OFF) : "memory", "scc");
}
template <int OFF> DI void spin_all_ge(unsigned vs4, unsigned need) {
  unsigned tv, cnt;
  asm volatile("s_mov_b32 %1, 0x40000\n.Lsp%=:\n\tds_read_b32 %0, %2 offset:%4\n\ts_waitcnt lgkmcnt(0)\n\tv_cmp_gt_u32 vcc, %3, %0\n\t"
               "s_cbranch_vccz .Lsd%=\n\ts_sub_u32 %1, %1, 1\n\ts_cbranch_scc0 .Lsp%=\n.Lsd%=:"
               : "=&v"(tv), "=&s"(cnt) : "v"(vs4), "s"(need), "i"(OFF - SY_OFF) : "memory", "scc", "vcc");
}
DI void wait_landed(unsigned vs, unsigned& have, unsigned need) {
  unsigned t0, t1, cnt;
  asm volatile("s_cmp_ge_u32 %0, %5\n\ts_cbranch_scc0 .Lslow%=\n.Lback%=:\n\t.subsection 1\n.Lslow%=:\n\ts_mov_b32 %3, 0x40000\n.Lsp%=:\n\t"
               "ds_read_b32 %1, %4 offset:%6\n\tds_read_b32 %2, %4 offset:%6+4\n\ts_waitcnt lgkmcnt(0)\n\tv_min_u32 %1, %1, %2\n\ts_nop 0\n\tv_readfirstlane_b32 %0, %1\n\t"
               "s_cmp_ge_u32 %0, %5\n\ts_cbranch_scc1 .Lback%=\n\ts_sub_u32 %3, %3, 1\n\ts_cbranch_scc0 .Lsp%=\n\ts_branch .Lback%=\n\t.subsection 0"
               : "+s"(have), "=&v"(t0), "=&v"(t1), "=&s"(cnt) : "v"(vs), "s"(need), "i"(SY_LANDED - SY_OFF) : "memory", "scc");
}
template <int OFF> DI void sig(unsigned vs, unsigned val) {
  asm volatile("s_mov_b64 exec, 1\n\tds_write_b32 %0, %1 offset:%2\n\ts_mov_b64 exec, -1" :: "v"(vs), "v"(val), "i"(OFF - SY_OFF) : "memory");
}
DI void sigq(unsigned vq, unsigned val) { asm volatile("ds_write_b32 %0, %1" :: "v"(vq), "v"(val) : "memory"); }

DI unsigned peek(unsigned addr, int off) { return *(volatile LAS unsigned*)(size_t)(addr + off - SY_OFF); }
template <int OFF> DI void need_all(unsigned seen, unsigned vs4, unsigned need) {
  unsigned tv, cnt;
  asm volatile("v_cmp_gt_u32 vcc, %3, %5\n\ts_cbranch_vccnz .Lslow%=\n.Lback%=:\n\t.subsection 1\n.Lslow%=:\n\ts_mov_b32 %1, 0x40000\n.Lsp%=:\n\t"
               "ds_read_b32 %0, %2 offset:%4\n\ts_waitcnt lgkmcnt(0)\n\tv_cmp_gt_u32 vcc, %3, %0\n\t"
               "s_cbranch_vccz .Lback%=\n\ts_sub_u32 %1, %1, 1\n\ts_cbranch_scc0 .Lsp%=\n\ts_branch .Lback%=\n\t.subsection 0"
               : "=&v"(tv), "=&s"(cnt) : "v"(vs4), "s"(need), "i"(OFF - SY_OFF), "v"(seen) : "memory", "scc", "vcc");
}
DI v2u peek_landed(unsigned vs) { return *(volatile LAS v2u*)(size_t)(vs + SY_LANDED - SY_OFF); }
DI void take_landed(unsigned& have, v2u v) { asm volatile("s_max_u32 %0, %0, %1" : "+s"(have) : "s"(rfl(min(v.x, v.y))) : "scc"); }
DI void publish(unsigned vsw, unsigned ev) { DRAIN(); sig<SY_PROG>(vsw, ev); }

DI v4u ln_stats(const v4u (&st)[4]) {
  f32x2 s = {0.f, 0.f};
#pragma unroll
  for (int i = 0; i < 4; ++i) s += f32x2{asf(st[i].x), asf(st[i].z)};
  const f32x2 mean = f32x2{sum32(sum16(s.x)), sum32(sum16(s.y))} * sp(1.f / 512.f);
  f32x2 m2 = {0.f, 0.f};
#pragma unroll
  for (int i = 0; i < 4; ++i) { const f32x2 d = fma2(f32x2{asf(st[i].x), asf(st[i].z)}, sp(1.f / 32.f), -mean); m2 += fma2(d * sp(32.f), d, f32x2{asf(st[i].y), asf(st[i].w)}); }
  const float v0 = sum32(sum16(m2.x)) * (1.f / 512.f), v1 = sum32(sum16(m2.y)) * (1.f / 512.f);
  const float sd0 = rbf(__builtin_amdgcn_sqrtf(rbf(rbf(v0) + 1e-5f))), sd1 = rbf(__builtin_amdgcn_sqrtf(rbf(rbf(v1) + 1e-5f)));
  return v4u{asu(__builtin_amdgcn_rcpf(sd0)), asu(-rbf(mean.x)), asu(__builtin_amdgcn_rcpf(sd1)), asu(-rbf(mean.y))};
}
struct LnSteps {
  v2u st[8]; f32x2 a, b, c, d; float S, R, m16, rstd, nmean, sd;
  static constexpr int N = 22;
  DI f32x2 f(int i) const { return f32x2{asf(st[i].x), asf(st[i].y)}; }
  template <int I> DI void step() {
    if constexpr (I == 0) { a.x = add3(asf(st[0].x), asf(st[1].x), asf(st[2].x)); b.x = add3(asf(st[3].x), asf(st[4].x), asf(st[5].x)); }
    else if constexpr (I == 1) { a.y = add3(asf(st[0].y), asf(st[1].y), asf(st[2].y)); b.y = add3(asf(st[3].y), asf(st[4].y), asf(st[5].y)); }
    else if constexpr (I == 2) { a.x = add3(add3(a.x, asf(st[6].x), asf(st[7].x)), b.x, 0.f); }
    else if constexpr (I == 3) { a.y = add3(add3(a.y, asf(st[6].y), asf(st[7].y)), b.y, 0.f); }
    else if constexpr (I == 4) { S = sum32(a.x); }
    else if constexpr (I == 5) { m16 = S * (1.f / 16.f); nmean = S * (-1.f / 512.f); }
    else if constexpr (I == 6) { const float e = asf(st[0].x) - m16; R = e * e; }
    else if constexpr (I < 14) { const float e = asf(st[I - 6].x) - m16; R = __builtin_fmaf(e, e, R); }
    else if constexpr (I == 14) { R = __builtin_fmaf(R, 1.f / 32.f, a.y); }
    else if constexpr (I == 15) { R = sum32(R); }
    else if constexpr (I == 16) { R = rbf(R * (1.f / 512.f)); }
    else if constexpr (I == 17) { R = rbf(R + 1e-5f); }
    else if constexpr (I == 18) { R = __builtin_amdgcn_sqrtf(R); }
    else if constexpr (I == 19) { R = rbf(R); }
    else if constexpr (I == 20) { sd = R; rstd = __builtin_amdgcn_rcpf(R); }
    else if constexpr (I == 21) { nmean = rbf(nmean); }

    if constexpr (I == 4) asm volatile("" : "+v"(S)); if constexpr (I == 5) asm volatile("" : "+v"(m16), "+v"(nmean));
    if constexpr (I >= 6 && I < 20) asm volatile("" : "+v"(R)); if constexpr (I == 20) asm volatile("" : "+v"(rstd)); if constexpr (I == 21) asm volatile("" : "+v"(nmean));
  }
};
DI v4u ldx(__amdgpu_buffer_rsrc_t rs, int voff, int soff) { return __builtin_amdgcn_raw_buffer_load_b128(rs, voff, soff, AUX_SC1); }
DI v2u ldx2(__amdgpu_buffer_rsrc_t rs, int voff, int soff) { return __builtin_amdgcn_raw_buffer_load_b64(rs, voff, soff, AUX_SC1); }

#define MAPB 1
#define AB 0
#define DEPTH1 20
#define NE1 0
#define W1GATE SY_SWEPT
DI void stx(v4u v, __amdgpu_buffer_rsrc_t rs, int voff) { __builtin_amdgcn_raw_buffer_store_b128(v, rs, voff, 0, AUX_SC1); }
DI void stx2(v2u v, __amdgpu_buffer_rsrc_t rs, int voff) { __builtin_amdgcn_raw_buffer_store_b64(v, rs, voff, 0, AUX_SC1); }
#define CS3E 8
#define G2WORD SY_GO2
#define GATEN 60
#define E1 8
#define E3 8
#define PFD 6
#define LATE0 28
#define XW 8
#define DEPTH 12
struct Loader {
  unsigned vsl  , vs6  , vs, cons; int pub;
#define LOOK "ds_read_b32 %3, %4 offset:%7\n\ts_waitcnt lgkmcnt(0)\n\t" \
             "v_min_u32_dpp %3, %3, %3 row_shr:2 row_mask:0xf bank_mask:0xf\n\ts_nop 1\n\t" \
             "v_min_u32_dpp %3, %3, %3 row_shr:1 row_mask:0xf bank_mask:0xf\n\ts_nop 0\n\t" \
             "v_readlane_b32 %0, %3, 3\n\ts_cmp_ge_u32 %0, %5\n\ts_cbranch_scc1 .Lback%=\n\t"
#define RUNG(N) "s_waitcnt vmcnt(" #N ")\n\ts_sub_i32 %1, %8, 2 * " #N "\n\ts_max_i32 %2, %2, %1\n\tv_mov_b32 %3, %2\n\tds_write_b32 %6, %3\n\t" LOOK
  unsigned tmp;
  DI void room(unsigned pos, unsigned end) {
    unsigned cnt;
    asm volatile("s_cmp_ge_i32 %0, %5\n\ts_cbranch_scc0 .Lslow%=\n.Lback%=:\n\t.subsection 1\n.Lslow%=:\n\t" LOOK RUNG(8) RUNG(4)
                 "s_waitcnt vmcnt(0)\n\tv_mov_b32 %3, %8\n\ts_mov_b32 %2, %8\n\tds_write_b32 %6, %3\n\ts_mov_b32 %1, 0x40000\n.Lsp%=:\n\t" LOOK
                 "s_sleep 1\n\ts_sub_u32 %1, %1, 1\n\ts_cbranch_scc0 .Lsp%=\n\ts_branch .Lback%=\n\t.subsection 0"
                 : "+s"(cons), "=&s"(cnt), "+s"(pub) : "v"(tmp), "v"(vs6), "s"(end - NR), "v"(vsl), "i"(SY_CONS - SY_OFF), "s"(pos) : "memory", "scc");
  }
  unsigned hv, rv;
  DI void look() { asm volatile("ds_read_b32 %0, %2 offset:%3\n\tds_read_b32 %1, %2 offset:%4" : "=&v"(hv), "=&v"(rv) : "v"(vs), "i"(SY_HOLD - SY_OFF), "i"(SY_READY - SY_OFF) : "memory"); }
  DI void done(unsigned end) {
    pub = max((int)end - 2 * DEPTH, pub);
    unsigned cnt;
    asm volatile("s_waitcnt vmcnt(%7) lgkmcnt(0)\n\tds_write_b32 %4, %5\n\tv_cmp_gt_u32 vcc, %2, %3\n\ts_cbranch_vccnz .Lslow%=\n.Lback%=:\n\t.subsection 1\n.Lslow%=:\n\t"
                 "s_waitcnt vmcnt(0)\n\tv_mov_b32 %2, %6\n\ts_mov_b32 %0, %6\n\tds_write_b32 %4, %2\n\ts_mov_b32 %1, 0x40000\n.Lsp%=:\n\t"
                 "ds_read_b32 %2, %8 offset:%9\n\tds_read_b32 %3, %8 offset:%10\n\ts_waitcnt lgkmcnt(0)\n\tv_cmp_gt_u32 vcc, %2, %3\n\ts_cbranch_vccz .Lback%=\n\t"
                 "s_sub_u32 %1, %1, 1\n\ts_cbranch_scc0 .Lsp%=\n\t"
                 "s_branch .Lback%=\n\t.subsection 0"
                 : "+s"(pub), "=&s"(cnt) : "v"(hv), "v"(rv), "v"(vsl), "v"(pub), "s"(end), "i"(DEPTH), "v"(vs), "i"(SY_HOLD - SY_OFF), "i"(SY_READY - SY_OFF) : "memory", "scc", "vcc");
  }
};
#define GL0(v, rs, so, c) "buffer_load_dwordx4 " v ", " rs ", " so " offen" c " lds\n\t"
#define GL1(v, rs, so, c) "s_nop 0\n\t" GL0(v, rs, so, c)
#define GLN(v, rs, so, c) "s_add_u32 m0, m0, 0x800\n\t" GL1(v, rs, so, c)
DI v4u srd(const void* base, unsigned bytes) { const u64 a = (u64)base; return v4u{(unsigned)a, (unsigned)(a >> 32) & 0xFFFFu, bytes, 0x00020000u}; }
DI void dma1(unsigned lds, unsigned voff, v4u r, unsigned so) { asm volatile("s_mov_b32 m0, %0\n\t" GL1("%1", "%2", "%3", "") :: "s"(lds), "v"(voff), "s"(r), "s"(so) : "memory"); }
DI int wg_of(int tt, int jj) { return ((tt & 3) * 8 + (jj & 7)) * 8 + (tt >> 2) * 2 + (jj >> 3); }
DI void frag_out(s16x8 f, __amdgpu_buffer_rsrc_t rs, int off, float& cs) {
  const v4u v = __builtin_bit_cast(v4u, f); const unsigned ones = 0x3f803f80u;
  stx(v, rs, off);
  cs = dot2(v.w, ones, dot2(v.z, ones, dot2(v.y, ones, dot2(v.x, ones, cs))));
}
constexpr int STG1 = 96 * 1024, STG2 = 104 * 1024, STG4 = 112 * 1024, STG3 = 120 * 1024;

DI void prep3(const Params& p, LAS char* L, unsigned lb, unsigned vs, __amdgpu_buffer_rsrc_t rs, int lane, int t, int j, int l) {
  const int h = lane >> 5, gi = (lane >> 4) & 1, q = (lane & 15) >> 2, pp = lane & 3, us = (j / 3) * 16 + (j % 3) * 6, ks = 2 * t + l;
  LAS char* bE = L + STG3 + l * 6144 + (4 * h + q) * 192 + 8 * pp + gi * (3072 + 32); LAS char* bO = bE - gi * 64;
  const v4u rg = srd(p.wg, 512 * 2752), ru = srd(p.wu, 512 * 2752);
  spin_all_ge<SY_RAW>(vs + 4 * (lane & 3), 1u);
#pragma unroll
  for (int i = 0; i < 6; ++i) {
    const int e = 64 * (i % 3) + lane, r = (e * 43691) >> 19, c = e - 12 * r, up = i / 3;
    dma1(lb + STG3 + (6 * l + i) * 1024, r * 2752 + (c ^ (2 * up)) * 16 + 32 * us, up ? ru : rg, ks * (16 * 2752));
  }
  DRAIN();
  s16x8 f[6];
#pragma unroll
  for (int u = 0; u < 6; ++u) { LAS char* b = ((u & 1) ? bO : bE) + 32 * u; f[u] = cat(trr(b), trr(b + 1536)); }
  sig<SY_SR>(vs + 4 * l, 1u);
  float cs[6];
#pragma unroll
  for (int u = 0; u < 6; ++u) { cs[u] = 0.f; frag_out(f[u], rs, WT3_OFF + (j * 192 + 6 * ks + u) * 1024 + lane * 16, cs[u]); }
#pragma unroll
  for (int u = 0; u < 6; u += 2) {
    auto r = __builtin_amdgcn_permlane32_swap(asu(cs[u]), asu(cs[u + 1]), false, false);
    __builtin_amdgcn_raw_buffer_store_b32(asu(asf(r[0]) + asf(r[1])), rs, CSP_OFF + ((j * 32 + ks) * 288 + 96 + 32 * u + lane) * 4, 0, AUX_SC1);
  }
  DRAIN();
}

DI void loader_main(const Params& p, LAS char* L, unsigned lb, int lane, int t, int j, int l, int wave) {
  const __amdgpu_buffer_rsrc_t rs = __builtin_amdgcn_make_buffer_rsrc((void*)p.ws, 0, WS_BYTES, 0x00020000);
  Loader ld; ld.vsl = lane == 0 ? lb + SY_LANDED + 4 * l : lb + SY_DUMMY + 4 * lane; ld.vs6 = lb + SY_OFF + 4 * (lane & 3);
  ld.cons = opq(0); ld.pub = opq(0); ld.vs = lb + SY_OFF; asm volatile("v_mov_b32 %0, 0" : "=v"(ld.tmp));
  auto fresh = [&]() { int x = lane; asm volatile("" : "+v"(x)); return x; };
  const int jc = j >> 1, idx = 2 * t + (j & 1);
  auto off2 = [&]() { const int x = fresh(); return (unsigned)((x >> 3) * 1024 + (((x & 7) ^ (((x >> 4) & 1) * 4)) * 16) + 128 * jc); };
  auto rd2 = [&](int stg, int c) {
    const int x = fresh(), h = x >> 5, gi = (x >> 4) & 1, q = (x & 15) >> 2, pp = x & 3;
    LAS char* b = L + stg + (((4 * h + q) * 128 + ((2 * gi + (pp >> 1)) ^ (4 * ((q >> 1) & 1))) * 16 + 8 * (pp & 1)) ^ (64 * c));
    return __builtin_bit_cast(v4u, cat(trr(b), trr(b + 1024)));
  };
  if (l == 0) {
    const v4u ro = srd(p.wo, 512 * 1024); const unsigned o2 = off2();
    dma1(lb + STG2, o2, ro, idx * 16384); dma1(lb + STG2 + 1024, o2 + 8192, ro, idx * 16384);
    DRAIN();
#pragma unroll
    for (int c = 0; c < 2; ++c) stx(rd2(STG2, c), rs, WT2_OFF + (jc * 64 + 4 * (idx & 15) + 2 * (idx >> 4) + c) * 1024 + lane * 16);
    DRAIN(); sig<SY_T2>(ld.vs, 1u);
  }
  const v4u rw = srd(p.ws, WS_BYTES);
  const unsigned lbl = lb + l * 1024;
  auto seg = [&](unsigned so, unsigned pos, int n) {
    const unsigned v0 = fresh() * 16 + l * 1024;
    unsigned m = lbl + (pos % NR) * 1024;
#pragma nounroll
    for (int i = 0; i < n; ++i) {
      ld.room(pos, pos + 8);
      ld.look();
      asm volatile("s_mov_b32 m0, %0\n\t" GL1("%1", "%5", "%6", " sc1") GLN("%2", "%5", "%6", " sc1") GLN("%3", "%5", "%6", " sc1") GLN("%4", "%5", "%6", " sc1")
                   :: "s"(m), "v"(v0), "v"(v0 + 2048), "v"(v0 + 4096), "v"(v0 + 6144), "s"(rw), "s"(so) : "memory", "scc");
      pos += 8; so += 8192; m += 8192; if (m >= lbl + NR * 1024) m -= NR * 1024;
      ld.done(pos);
    }
  };
  v4u w1[8][6];
  LAS char* L1; LAS char* L1b;
  auto base1 = [&]() { L1 = (LAS char*)(size_t)(lb + fresh() * 16 + l * 6144); L1b = opqv(L1 + 49152); };
  {
    spin_ge<SY_WT>(ld.vs, 1);
    STAMP(16);
    const unsigned v0 = fresh() * 16 + l * 1024;
    unsigned m = lbl, so = WT1_OFF + j * 96 * 1024;
#pragma nounroll
    for (int i = 0; i < 12; ++i) {
      asm volatile("s_mov_b32 m0, %0\n\t" GL1("%1", "%5", "%6", " sc1") GLN("%2", "%5", "%6", " sc1") GLN("%3", "%5", "%6", " sc1") GLN("%4", "%5", "%6", " sc1")
                   "s_waitcnt vmcnt(%7)\n\tds_write_b32 %8, %9"
                   :: "s"(m), "v"(v0), "v"(v0 + 2048), "v"(v0 + 4096), "v"(v0 + 6144), "s"(rw), "s"(so), "i"(DEPTH1), "v"(ld.vsl), "v"(max(8 * i + 8 - 2 * DEPTH1, 0)) : "memory", "scc");
      so += 8192; m += 8192;
    }
    STAMP(17);
  }
  {
    asm volatile("s_waitcnt vmcnt(12)" ::: "memory"); sigq(ld.vsl, 72);
    DRAIN(); sigq(ld.vsl, 73);
    STAMP(20);
    spin_all_ge<SY_LANDED>(ld.vs + 4 * (lane & 1), 73);
    base1();
    sfor<0, 8>([&](auto I) {
#pragma unroll
      for (int q = 0; q < 6; ++q) w1[I.v][q] = *(LAS v4u*)((I.v < 4 ? L1 : L1b) + (I.v & 3) * 12288 + q * 1024);
      SBAR();
    });
    ld.pub = opq(P_W2); sigq(ld.vsl, ld.pub);
    STAMP(21);
  }
  seg(WT2_OFF + jc * 64 * 1024, P_W2, 2);
  {
    const v4u rd = srd(p.wd, 1376 * 1024); const unsigned o2 = off2();
    spin_all_ge<SY_RAW>(ld.vs + 4 * (lane & 3), 1u);
#pragma unroll
    for (int i = 0; i < 4; ++i) if (i < 2 || l == 0) dma1(lb + STG4 + l * 4096 + i * 1024, o2 + (i & 1) * 8192, rd, (idx + 32 * (i < 2 ? l : 2)) * 16384);
    DRAIN(); ld.pub = opq(P_W2 + 16); sigq(ld.vsl, ld.pub);
    v4u f[4];
#pragma unroll
    for (int i = 0; i < 4; ++i) f[i] = rd2(STG4 + l * 4096 + (i >> 1) * 2048, i & 1);
    sig<SY_SR + 8>(ld.vs + 4 * l, 1u);
#pragma unroll
    for (int i = 0; i < 2; ++i) {
      const int u = idx + 32 * (i ? 2 : l), kh = u >= 43, r16 = u & 15, jp = 3 * (u >> 4) + (r16 >= 12 ? 2 : r16 >= 6), r = r16 - (r16 >= 12 ? 12 : (r16 >= 6 ? 6 : 0)), jr = jp - 8 * kh;
      const int a = (r < 3 && jr >= 0) ? 3 * jr + r : 24 + u - 43 * kh - 3 * (jr + 1);
      if (u < 86 && (i == 0 || l == 0)) {
#pragma unroll
        for (int c = 0; c < 2; ++c) stx(f[2 * i + c], rs, WT4_OFF + (jc * 172 + 4 * a + 2 * kh + c) * 1024 + lane * 16);
      }
    }
    DRAIN(); sig<SY_TC + 4>(ld.vs + 4 * l, 1u);
    STAMP(22);
    spin_all_ge<SY_SR>(ld.vs + 4 * (lane & 3), 1u);
  }
#pragma nounroll
  for (int pass = 0; pass < 2; ++pass) {
    const unsigned pb = pass * P_END;
    STAMP(40 + 8 * pass);
    seg(WT2_OFF + (jc * 64 + 16 - 16 * pass) * 1024, pb + P_W2 + 16 - 16 * pass, 6 + 2 * pass);
    STAMP(41 + 8 * pass);
    if (pass == 0) { DRAIN(); ld.pub = opq(P_W3); sigq(ld.vsl, ld.pub); spin_ge<SY_WT>(ld.vs, 2); }
    seg(WT3_OFF + j * 192 * 1024, pb + P_W3, 24);
    STAMP(42 + 8 * pass);
    seg(WT4_OFF + jc * 172 * 1024, pb + P_W4, T4 / 2);
    DRAIN(); ld.pub = opq(pb + P_END); sigq(ld.vsl, ld.pub);
    STAMP(43 + 8 * pass);
    if (pass == 1) break;
    base1();
    sfor<0, 8>([&](auto I) {
      const unsigned pos = P_END + 12 * I.v + 6 * l;
      ld.room(pos, pos + 6);
#pragma unroll
      for (int q = 0; q < 6; ++q) *(LAS v4u*)((I.v < 4 ? L1 : L1b) + (I.v & 3) * 12288 + q * 1024) = w1[I.v][q];
      ld.pub = opq(I.v < 7 ? pos + 12 : P_END + P_W2); sigq(ld.vsl, ld.pub);
    });
  }
}

struct Tail {
  v4u r[2 * (43 - T4)];
  DI void load(__amdgpu_buffer_rsrc_t rs, unsigned vs, int lane, int j, int e) {
    spin_ge<SY_WT>(vs, 2);
#pragma unroll
    for (int i = 0; i < 2 * (43 - T4); ++i) r[i] = ldx(rs, lane * 16 + (i & 1) * 1024, WT4_OFF + ((j >> 1) * 172 + 4 * (T4 + (i >> 1)) + 2 * e) * 1024);
    SBAR();
  }
  DI void put(LAS char* L, unsigned vs, int lane, int e, int pass) {
    spin_all_ge<SY_CONS>(vs + 4 * (lane & 3), pass * P_END + pos4(43) - NR);
#pragma unroll
    for (int i = 0; i < 2 * (43 - T4); ++i) *(LAS v4u*)(L + slot_of(pos4(T4)) + (4 * (i >> 1) + (i & 1)) * 1024 + e * 2048 + lane * 16) = r[i];
    sig<SY_T4>(vs + 4 * e, pass + 1);
  }
};
DI void helper_main(const Params& p, LAS char* L, unsigned lb, unsigned vs, int lane, int t, int j) {
  const int wave = 5; (void)wave;
  const __amdgpu_buffer_rsrc_t rs = __builtin_amdgcn_make_buffer_rsrc((void*)p.ws, 0, WS_BYTES, 0x00020000);
  const int h = lane >> 5, gi = (lane >> 4) & 1, q = (lane & 15) >> 2, pp = lane & 3;
  {
    const v4u rq = srd(p.wqkv, 512 * 3072);
    const unsigned o1 = (lane >> 2) * 3072 + (lane & 3) * 16 + 64 * j;
#pragma unroll
    for (int i = 0; i < 6; ++i) dma1(lb + STG1 + i * 1024, o1 + (i % 3) * 1024, rq, (2 * t + i / 3) * (16 * 3072));
    const u64 e_ = __hip_atomic_load((GAS u64*)(p.ws + EPOCH_OFF), RLX_AGENT);
    asm volatile("s_waitcnt vmcnt(1)" ::: "memory");
    STAMP(12);
    LAS char* b1 = L + STG1 + (4 * h + q) * 64 + gi * 32 + pp * 8;
#pragma unroll
    for (int i = 0; i < 6; ++i) {
      const int ks = 2 * t + i / 3, c = (i % 3 + 2) % 3;
      const int pos = ks < QA ? 3 * ks + c : (c < 2 ? 3 * QA + 2 * (ks - QA) + c : 64 + ks);
      float cs = 0.f;
      frag_out(cat(trr(b1 + i * 1024), trr(b1 + i * 1024 + 512)), rs, WT1_OFF + (j * 96 + pos) * 1024 + lane * 16, cs);
      cs = sum32(cs);
      if (lane < 32) __builtin_amdgcn_raw_buffer_store_b32(asu(cs), rs, CSP_OFF + ((j * 32 + ks) * 288 + 32 * (i % 3) + lane) * 4, 0, AUX_SC1);
    }
    DRAIN();
    const u64 E = uni64(e_);
    GAS u64* mine = (GAS u64*)(p.ws + CSF_OFF + blockIdx.x * 256);
    spin_ge<SY_T2>(vs, 1u);
    if (lane == 0) __hip_atomic_store(mine, E + 1, RLX_AGENT);
    auto poll = [&](GAS u64* f, int n, u64 want) {
      for (unsigned k = 0; k < (1u << 16); ++k) {
        u64 v = want;
        if (lane < n) v = __hip_atomic_load(f, RLX_AGENT);
        if (__all(v - want <= E + 2 - want)) break;
      }
      __builtin_amdgcn_fence(__ATOMIC_ACQUIRE, "wavefront");
    };
    GAS u64* f = (GAS u64*)(p.ws + CSF_OFF + wg_of(lane & 15, j ^ ((lane >> 4) & 1)) * 256);
    poll(f, 32, E + 1);
    sig<SY_WT>(vs, 1u);
    STAMP(13);
    {
      const int l2 = lane < 48 ? lane : lane - 48, hi = l2 >= 24;
      v4u g[16];
#pragma unroll
      for (int i = 0; i < 16; ++i) g[i] = ldx(rs, hi * 1152 + (l2 - 24 * hi) * 16, CSP_OFF + (j * 32 + 2 * i) * 1152);
      SBAR();
      f32x4 tot = __builtin_bit_cast(f32x4, g[0]);
#pragma unroll
      for (int i = 1; i < 16; ++i) tot += __builtin_bit_cast(f32x4, g[i]);
#pragma unroll
      for (int i = 0; i < 4; ++i) tot[i] += asf(__builtin_amdgcn_ds_bpermute((lane + 24) * 4, asu(tot[i])));
      if (lane < 24) *(LAS f32x4*)(L + CS1_OFF + lane * 16) = tot;
      sig<SY_CS>(vs, 1u);
      STAMP(15);
    }
    prep3(p, L, lb, vs, rs, lane, t, j, 1);
    spin_all_ge<SY_TC>(vs + 4 * min(lane & 3, 2), 1u);
    STAMP(17);
    if (lane == 0) __hip_atomic_store(mine, E + 2, RLX_AGENT);
    poll(f, 32, E + 2);
    sig<SY_WT>(vs, 2u);
    STAMP(14);
    {
      f32x4 tot = {0.f, 0.f, 0.f, 0.f};
#pragma unroll
      for (int k = 0; k < 2; ++k) {
        v4u g[16];
#pragma unroll
        for (int i = 0; i < 16; ++i) g[i] = ldx(rs, 384 + min(lane, 47) * 16, CSP_OFF + (j * 32 + 16 * k + i) * 1152);
        SBAR();
#pragma unroll
        for (int i = 0; i < 16; ++i) tot += __builtin_bit_cast(f32x4, g[i]);
      }
      if (lane < 48) *(LAS f32x4*)(L + CS3_OFF + lane * 16) = tot;
      sig<SY_CS>(vs, 2u);
    }
  }
  spin_ge<SY_READY>(vs, 3u);
  Tail tl; tl.load(rs, vs, lane, j, 1);
  tl.put(L, vs, lane, 1, 0);
  tl.put(L, vs, lane, 1, 1);
}

DI void poller_main(const Params& p, LAS char* L, unsigned vs, int lane, int t, int j, int wave) {
  const __amdgpu_buffer_rsrc_t rs = __builtin_amdgcn_make_buffer_rsrc((void*)p.ws, 0, WS_BYTES, 0x00020000);
  const u64 E = uni64(__hip_atomic_load((GAS u64*)(p.ws + EPOCH_OFF), RLX_AGENT));
  const int own = wg_of(t, lane & 15), prev = wg_of(t > 0 ? t - 1 : 0, lane & 15);
  auto hop = [&](unsigned ev, bool halo) {
    spin_all_ge<SY_PROG>(vs + 4 * (lane & 3), ev);
    if (lane == 0) __hip_atomic_store((GAS u64*)(p.ws + FLAG_OFF + blockIdx.x * 256), E + ev, RLX_AGENT);
    GAS u64* f = (GAS u64*)(p.ws + FLAG_OFF + ((halo && lane >= 16) ? prev : own) * 256);
    for (unsigned n = 0; n < (1u << 16); ++n) {
      u64 v = E + ev;
      if (lane < (halo ? 32 : 16)) v = __hip_atomic_load(f, RLX_AGENT);
      if (__all(v - (E + ev) <= (u64)(10u - ev))) break;
    }
    __builtin_amdgcn_fence(__ATOMIC_ACQUIRE, "wavefront");
    sig<SY_READY>(vs, ev);
  };
  Tail tl;
  sfor<0, 2>([&](auto PASS) {
    constexpr int pass = PASS.v;
    hop(1 + 5 * pass, true);
    STAMP(1 + 4 * pass);
    {
      const int hb = t > 0 ? 4 * t - 1 : 0, G = lane >> 4;
      const int hvo = (G >> 1) * 1024 + (16 + (lane & 15) + 32 * (G & 1)) * 16, xb = X_OFF + 2 * pass * X_SZ + hb * 32768;
      v4u a[4], hx[16];
#pragma unroll
      for (int i = 0; i < 4; ++i) a[i] = ldx(rs, lane * 16 + i * 1024, ST_OFF + 2 * pass * ST_SZ + hb * 4096);
#pragma unroll
      for (int i = 0; i < 16; ++i) hx[i] = ldx(rs, hvo + (i & 1) * 2048, xb + (i >> 1) * 4096);
      SBAR();
      const v4u r = ln_stats(a);
      if (lane >= 8 && lane < 16) *(LAS v4u*)(L + LN_OFF + (2 * lane - 16) * 8) = r;
#pragma unroll
      for (int i = 0; i < ((AB & 4) ? 1 : 16); ++i) *(LAS v4u*)(L + EX_OFF + i * 1024 + lane * 16) = hx[i];
      sig<SY_LN>(vs, pass + 1);
    }
    if constexpr (pass == 0) { prep3(p, L, (unsigned)(size_t)L, vs, rs, lane, t, j, 0); sig<SY_TC>(vs, 1u); }
#pragma nounroll
    for (unsigned e = 2; e <= 5; ++e) {
      hop(e + 5 * pass, false);
      if (e != 5) STAMP(e + 4 * pass); else STAMP(62 + pass);
      if (pass == 0 && e == 3) tl.load(rs, vs, lane, j, 0);
    }
    tl.put(L, vs, lane, 0, pass);
  });
  if (blockIdx.x == 0) {
    for (unsigned n = 0; n < (1u << 16); ++n) {
      bool ok = true;
#pragma unroll
      for (int i = 0; i < 4; ++i) ok &= (__hip_atomic_load((GAS u64*)(p.ws + FLAG_OFF + (i * 64 + lane) * 256), RLX_AGENT) - (E + 2) <= 8ull);
      if (__all(ok)) break;
    }
    if (lane == 0) __hip_atomic_store((GAS u64*)(p.ws + EPOCH_OFF), E + 16, RLX_AGENT);
  }
}

template <int S> DI v4u ldf(__amdgpu_buffer_rsrc_t rs, int voff, int base) { return ldx(rs, voff + (S & 3) * 1024, base + (S >> 2) * 4096); }
struct LB { LAS char* b[3]; DI explicit LB(LAS char* x) { b[0] = opqv(x); b[1] = opqv(x + 60 * 1024); b[2] = opqv(x + 120 * 1024); }
  template <int OFF> DI LAS char* at() const { return b[OFF / (60 * 1024)] + OFF % (60 * 1024); } };

#define HOLDMASK 0
template <int PH> DI void hold(unsigned vs, unsigned ev) { if constexpr ((HOLDMASK >> PH) & 1) sig<SY_HOLD>(vs, ev); }
struct Resid { v4u a, b; };
DI Resid ld_resid(__amdgpu_buffer_rsrc_t rs, int lane, int wave, int t, int j, int r_off) {
  const int ro = opq(r_off + ((4 * t + 2 * (j & 1) + (wave & 1)) * 32 + 4 * (j >> 1) + 2 * (wave >> 1)) * 1024);
  return Resid{ldf<0>(rs, lane * 16, ro), ldf<1>(rs, lane * 16, ro)};
}
template <int NKS, bool IS_DOWN>
DI void proj_phase(const Params& p, LAS char* L, unsigned vs, __amdgpu_buffer_rsrc_t rs, int lane, int wave, int t, int j, int pass, unsigned& have,
                   int a_off  , const Resid& res, int x_off, int st_off, unsigned ev  , unsigned ph) {
  const int m2 = wave & 1, kh = wave >> 1, th = j & 1, jc = j >> 1, tb = 4 * t + 2 * th + m2;
  const int h = lane >> 5;
  const unsigned pb = pass * P_END, vsw = vs + 4 * wave;
  const unsigned vq = lane == 0 ? vsw + (SY_CONS - SY_OFF) : vs + (SY_DUMMY - SY_OFF) + 4 * lane;
  f32x16 acc[2];
#pragma unroll
  for (int i = 0; i < 4; ++i) { acc[0][2 * i] = bflo(res.a[i]); acc[0][2 * i + 1] = bfhi(res.a[i]); acc[0][8 + 2 * i] = bflo(res.b[i]); acc[0][9 + 2 * i] = bfhi(res.b[i]); }
#pragma unroll
  for (int i = 0; i < 16; ++i) acc[1][i] = 0.f;
  asm volatile("" : "+v"(acc[0]));
  auto posk = [](int a) { return IS_DOWN ? pos4(a) : pos2(a); };
  const LB b0(L + lane * 16 + kh * 3072), b1(L + lane * 16 + kh * 1024 + 1024);
  auto rd = [&](auto N) -> s16x8 {
    constexpr int a = N.v >> 1, c = N.v & 1, sl = slot_of(IS_DOWN ? pos4(a) : pos2(a));
    const LB& b = c ? b1 : b0;
    return *(LAS s16x8*)b.template at<sl>();
  };
  constexpr int NOP = 2 * NKS, PF = PFD, LAST = IS_DOWN ? T4 : NKS;
  s16x8 wf[PF];
  wait_landed(vs, have, pb + posk(8));
  sfor<0, PF>([&](auto N) { wf[N.v] = rd(N); });
  const int ab = opq(a_off + tb * (IS_DOWN ? 86 : 32) * 1024 + (IS_DOWN ? 0 : kh * 16384)), khk = opq(kh * 1024);
  v4u xf[NKS];
  auto ldk = [&](auto K) {
    constexpr int u0 = ord4(0, K.v), u1 = ord4(1, K.v);
    if constexpr (IS_DOWN) xf[K.v] = ldx(rs, lane * 16, ab + u0 * 1024 + khk * (u1 - u0));
    else xf[K.v] = ldf<K.v>(rs, lane * 16, ab);
  };
  SBAR();
  spin_ge<SY_READY>(vs, ev + (IS_DOWN && LATE0 == 16 ? 1 : 0));
  STAMP(16 + 4 * ph);
  CDECL(); if (IS_DOWN) CST(4, 0);
  sfor<0, XW>([&](auto S) { ldk(S); });
  SBAR();
  v2u pl; unsigned pf;
  sfor<0, NOP>([&](auto N) {
    constexpr int n = N.v;
    const s16x8 cur = wf[n % PF];
    if constexpr (n + PF < NOP) {
      constexpr int a2 = (n + PF) >> 1;
      if constexpr (IS_DOWN && n + PF == 18) sigq(vq, pb + posk(9));
      if constexpr (((n + PF) & 15) == 10 && a2 + 3 < LAST) pl = peek_landed(vs);
      if constexpr (((n + PF) & 15) == 15 && a2 + 1 < LAST) take_landed(have, pl);
      if constexpr (((n + PF) & 15) == 0 && a2 < LAST) wait_landed(vs, have, pb + posk(a2 + 8 < LAST ? a2 + 8 : LAST));
      if constexpr (IS_DOWN && n + PF == 2 * T4 - 5) pf = peek(vs + 4 * (lane & 1), SY_T4);
      if constexpr (IS_DOWN && n + PF == 2 * T4 - 1) need_all<SY_T4>(pf, vs + 4 * (lane & 1), pass + 1);
      wf[n % PF] = rd(IC<n + PF>{});
    }
    acc[n & 1] = mfma32(cur, xf[n >> 1], acc[n & 1]);
    if constexpr (IS_DOWN && n == LATE0 - 5) pf = peek(vs, SY_READY);
    if constexpr (IS_DOWN && n == LATE0 - 1) need_all<SY_READY>(pf, vs, ev + 1);
    constexpr int k = XW + n < 24 ? XW + n : (n >= LATE0 ? 24 + n - LATE0 : NKS);
    if constexpr (k < NKS) ldk(IC<k>{});
    if constexpr (IS_DOWN) { if constexpr (n == 1) CST(4, 1); if constexpr (n == 15) CST(4, 2); if constexpr (n == 31) CST(4, 3); if constexpr (n == 47) CST(4, 4); if constexpr (n == 63) CST(4, 5); if constexpr (n == 75) CST(4, 6); if constexpr (n == 85) CST(4, 7); }
    SBAR();
  });
  asm volatile("" : "+v"(acc[0]), "+v"(acc[1]));
  sigq(vq, pb + (IS_DOWN ? P_END : posk(NKS)));
  const unsigned evo = ev + (IS_DOWN ? 2 : 1);
  if (!(IS_DOWN && pass == 1)) hold<IS_DOWN ? 4 : 2>(vs, evo);
  if (IS_DOWN) CDUMP(4);
  STAMP(17 + 4 * ph);
#pragma unroll
  for (int g = 0; g < 4; ++g) *(LAS f32x4*)(L + EX_OFF + wave * 4096 + g * 1024 + lane * 16) = f32x4{acc[1][4 * g], acc[1][4 * g + 1], acc[1][4 * g + 2], acc[1][4 * g + 3]};
  sig<SY_XF>(vsw, evo);
  spin_ge<SY_XF>(vs + 4 * (wave ^ 2), evo);
  unsigned o[8]; f32x2 xv[8];
#pragma unroll
  for (int g = 0; g < 4; ++g) {
    const f32x4 e = *(LAS f32x4*)(L + EX_OFF + (wave ^ 2) * 4096 + g * 1024 + lane * 16);
    xv[2 * g] = f32x2{acc[0][4 * g], acc[0][4 * g + 1]} + f32x2{e[0], e[1]};
    xv[2 * g + 1] = f32x2{acc[0][4 * g + 2], acc[0][4 * g + 3]} + f32x2{e[2], e[3]};
    o[2 * g] = pk(xv[2 * g]); o[2 * g + 1] = pk(xv[2 * g + 1]);
  }
  if (IS_DOWN && pass == 1) {
    GAS char* dst = (GAS char*)p.out + (size_t)(tb * 32 + (lane & 31)) * 1024 + (64 * jc + 32 * kh + 4 * h) * 2;
#pragma unroll
    for (int g = 0; g < 4; ++g) *(GAS v2u*)(dst + 16 * g) = v2u{o[2 * g], o[2 * g + 1]};
    STAMP(18 + 4 * ph);
    return;
  }
  const int xo = x_off + (tb * 32 + 4 * jc + 2 * kh) * 1024 + lane * 16;
  stx(v4u{o[0], o[1], o[2], o[3]}, rs, xo);
  stx(v4u{o[4], o[5], o[6], o[7]}, rs, xo + 1024);
  const f32x2 s2 = ((xv[0] + xv[1]) + (xv[2] + xv[3])) + ((xv[4] + xv[5]) + (xv[6] + xv[7]));
  const float sum = sum32(s2.x + s2.y);
  const f32x2 mi = sp(sum * (-1.f / 32.f));
  f32x2 q0 = {0.f, 0.f}, q1 = {0.f, 0.f};
#pragma unroll
  for (int i = 0; i < 4; ++i) { const f32x2 d0 = xv[2 * i] + mi, d1 = xv[2 * i + 1] + mi; q0 = fma2(d0, d0, q0); q1 = fma2(d1, d1, q1); }
  q0 += q1;
  const float m2s = sum32(q0.x + q0.y);
  if (lane < 32) stx2(v2u{asu(sum), asu(m2s)}, rs, lane * 8 + st_off + (tb * 16 + 2 * jc + kh) * 256);
  STAMP(18 + 4 * ph);
  publish(vsw, evo);
  STAMP(19 + 4 * ph);
}

DI void compute_main(const Params& p, LAS char* L, unsigned vs, int lane, int wave, int t, int j) {
  const __amdgpu_buffer_rsrc_t rs = __builtin_amdgcn_make_buffer_rsrc((void*)p.ws, 0, WS_BYTES, 0x00020000);
  unsigned have = 0;
  const unsigned vsw = vs + 4 * wave;
  STAMP(10);
  {
    const int c = lane >> 3, tk = lane & 7, odd = c & 1, blk = 4 * t + (j >> 2), sl = 8 * (j & 3) + tk;
    const GAS char* src = (const GAS char*)p.x + (size_t)(128 * t + 8 * j + tk) * 1024 + (16 * wave + c) * 16;
    v4u d[2];
    d[0] = *(const GAS v4u*)src; d[1] = *(const GAS v4u*)(src + 128);
    SBAR();
    STAMP(14);
#pragma unroll
    for (int a = 0; a < 2; ++a) {
      const int cI = 16 * wave + 8 * a + c;
      const unsigned r0 = ror8(odd ? d[a].x : d[a].z), r1 = ror8(odd ? d[a].y : d[a].w);
      const v4u o = odd ? v4u{r0, r1, d[a].z, d[a].w} : v4u{d[a].x, d[a].y, r0, r1};
      stx(o, rs, (cI >> 1) * 1024 + (sl + 32 * odd) * 16 + X_OFF + blk * 32768);
      float xv[8], s = 0.f;
#pragma unroll
      for (int i = 0; i < 4; ++i) { xv[2 * i] = bflo(d[a][i]); xv[2 * i + 1] = bfhi(d[a][i]); s += xv[2 * i] + xv[2 * i + 1]; }
      s += asf(ror8(asu(s))); s = sum16(s);
      const float mi = s * (1.f / 32.f); float m2 = 0.f;
#pragma unroll
      for (int i = 0; i < 8; ++i) { const float e = xv[i] - mi; m2 += e * e; }
      m2 += asf(ror8(asu(m2))); m2 = sum16(m2);
      if ((c & 3) == 0) stx2(v2u{asu(s), asu(m2)}, rs, (cI >> 2) * 256 + sl * 8 + ST_OFF + blk * 4096);
    }
    hold<0>(vs, 1);
    STAMP(11);
    publish(vsw, 1);
    STAMP(12);
  }
#pragma nounroll
  for (int pass = 0; pass < 2; ++pass) {
    const unsigned pb = pass * P_END, ph = 4 * pass + 1, eb = 5 * pass + 1;
    Resid res;
    const int xin = X_OFF + 2 * pass * X_SZ, stin = ST_OFF + 2 * pass * ST_SZ;
    const int tb = 4 * t + wave;
    {
      int ln = lane; asm volatile("" : "+v"(ln));
      const int h = ln >> 5, tok = ln & 31;
      CDECL();
      v4u xf[32], hx[16]; LnSteps lns;
      const int xb = opq(xin + tb * 32768), sb = opq(stin + tb * 4096);
      const int G = ln >> 4;
      const LB b1(L + ln * 16);
      f32x16 acc[3];
#pragma unroll
      for (int c = 0; c < 3; ++c)
#pragma unroll
        for (int i = 0; i < 16; ++i) acc[c][i] = 0.f;
      constexpr int PF = PFD;
      s16x8 wf[PF];
      const int oh = (16 * (wave >> 1) + (ln & 15) + 32 * (G & 1)) * 16 + (wave & 1) * 1024;
      const LB bh(L + oh + (G >> 1) * 3072);
      LAS char* bh2 = opqv(L + oh + (G >> 1) * 2048 + 3 * QA * 1024);
      auto rd = [&](auto N) -> s16x8 {
        constexpr int n = N.v;
        if constexpr (n >= 88 && n < 88 + QA / 2) return *(LAS s16x8*)bh.at<(n - 88) * 6144>();
        else if constexpr (n >= 88 && n < 104) return *(LAS s16x8*)(bh2 + (n - 88 - QA / 2) * 4096);
        else { constexpr int o = (n < 88 ? n : n - 16) * 1024; return *(LAS s16x8*)b1.at<o>(); }
      };
      f32x4 hacc = {0.f, 0.f, 0.f, 0.f};
      const unsigned vq = ln == 0 ? vsw + (SY_CONS - SY_OFF) : vs + (SY_DUMMY - SY_OFF) + 4 * ln, vs4 = vs + 4 * (ln & 3);
      LAS char* csb = opqv(L + CS1_OFF + 16 * h);
      LAS char* vw = opqv(L + EX_OFF + 2 * h * PLANE + (16 + 32 * wave + tok) * 16);
      LAS char* kw = opqv(L + KP_OFF + h * PLANE + (16 + 32 * wave + tok) * 16);
      LAS char* hxb = opqv(L + EX_OFF + ln * 16);
      wait_landed(vs, have, pb + 24);
      sfor<0, PF>([&](auto N) { wf[N.v] = rd(N); });
      SBAR();
      spin_ge<SY_READY>(vs, eb);
      STAMP(16 + 4 * ph);
      CST(5, 0);
      v2u pl; unsigned pf, pc; f32x4 cs[12]; f32x4 csh; f32x2 lnh;
      sfor<0, XW>([&](auto S) { xf[S.v] = ldf<S.v>(rs, ln * 16, xb); });
      SBAR();
      CST(5, 1);
      auto op = [&](auto N) {
        constexpr int n = N.v, r = n + PF;
        const s16x8 cur = wf[n % PF];
        if constexpr (r == 104) sigq(vq, pb + KP_OFF / 1024);
        if constexpr (r < 112) wf[n % PF] = rd(IC<r>{});
        constexpr int st = n < 3 * QA ? n / 3 : QA + (n - 3 * QA) / 2, c = n < 3 * QA ? (n % 3 + 1) % 3 : 1 + (n - 3 * QA) % 2;
        if constexpr (n < 88) acc[c] = mfma32(cur, xf[st], acc[c]);
        else if constexpr (n < 104) hacc = mfma16(cur, hx[n - 88], hacc);
        else acc[0] = mfma32(cur, xf[n - 80], acc[0]);
      };
      sfor<0, 104>([&](auto N) {
        constexpr int n = N.v, r = n + PF;
        if constexpr (r % 24 == 17 && r < 72) pl = peek_landed(vs);
        if constexpr (r % 24 == 23 && r < 72) take_landed(have, pl);
        if constexpr (r % 24 == 0 && r < 96) wait_landed(vs, have, pb + r + 24);
        op(N);
        {
          constexpr int k = n % 3 == 2 ? 32 : XW + 2 * (n / 3) + n % 3;
          if constexpr (k < 32) xf[k] = ldf<k>(rs, ln * 16, xb);
          if constexpr (n % 3 == 2 && n / 3 >= 4 && n / 3 < 12) lns.st[n / 3 - 4] = ldx2(rs, h * 2048 + tok * 8 + (n / 3 - 4) * 256, sb);
        }
        if constexpr (n == GATEN) sig<SY_RAW>(vsw, 1u);
        if constexpr (n >= 50 && n < 50 + LnSteps::N) lns.template step<n - 50>();
        if constexpr (n == 74) { pf = peek(vs, SY_LN); pc = peek(vs, SY_CS); }
        if constexpr (n == 79) need_all<SY_LN>(pf, vs, pass + 1);
        if constexpr (n == 81) need_all<SY_CS>(pc, vs, 1u);
        if constexpr (n >= 84 && n < 100) hx[n - 84] = *(LAS v4u*)(hxb + (n - 84) * 1024);
        if constexpr (n >= 100 && n < 104) cs[n - 100] = *(LAS f32x4*)(csb + (32 + 8 * (n - 100)) * 4);
        if constexpr (n == 2) CST(5, 2); if constexpr (n == 23) CST(5, 3); if constexpr (n == 47) CST(5, 4); if constexpr (n == 71) CST(5, 5); if constexpr (n == 87) CST(5, 6); if constexpr (n == 103) CST(5, 7);
        SBAR();
      });
      asm volatile("" : "+v"(acc[1]), "+v"(acc[2]), "+v"(hacc));
      STAMP(17 + 4 * ph);
      CST(1, 0);
      const f32x2 rstd = sp(lns.rstd), nmean = sp(lns.nmean);
      {
        unsigned pk_[8]; f32x2 vv[8];
        op(IC<104>{});
#pragma unroll
        for (int i = 4; i < 8; ++i) cs[i] = *(LAS f32x4*)(csb + (32 + 8 * i) * 4);
        csh = *(LAS f32x4*)(L + CS1_OFF + (32 * (1 + (wave & 1)) + 16 * (wave >> 1) + 4 * G) * 4); lnh = *(LAS f32x2*)(L + LN_OFF + (ln & 15) * 8);
        SBAR(); op(IC<105>{}); SBAR();
        sfor<0, 8>([&](auto I) { constexpr int i = I.v; pk_[i] = pk(fma2(f32x2{cs[i >> 1][2 * (i & 1)], cs[i >> 1][2 * (i & 1) + 1]}, nmean, pr<i>(acc[1])) * rstd); });
        pf = peek(vs4, SY_CONS);
        SBAR(); op(IC<106>{}); op(IC<107>{}); SBAR();
        sfor<0, 8>([&](auto I) { constexpr int i = I.v; vv[i] = fma2(f32x2{cs[4 + (i >> 1)][2 * (i & 1)], cs[4 + (i >> 1)][2 * (i & 1) + 1]}, nmean, pr<i>(acc[2])) * rstd; });
        const f32x2 hr = sp(t == 0 ? 0.f : lnh.x), hm = sp(t == 0 ? 0.f : lnh.y);
        f32x2 hv0 = fma2(f32x2{csh[0], csh[1]}, hm, f32x2{hacc[0], hacc[1]}) * hr, hv1 = fma2(f32x2{csh[2], csh[3]}, hm, f32x2{hacc[2], hacc[3]}) * hr;
        if (t == 0) { hv0 = sp(0.f); hv1 = sp(0.f); }
        SBAR();
        CST(1, 1);
        need_all<SY_CONS>(pf, vs4, pb + KP_OFF / 1024);
        op(IC<108>{});
        *(LAS v4u*)kw = v4u{pk_[0], pk_[1], pk_[2], pk_[3]};
        *(LAS v4u*)(kw + 2 * PLANE) = v4u{pk_[4], pk_[5], pk_[6], pk_[7]};
        SBAR(); op(IC<109>{}); SBAR();
        if (wave & 1) *(LAS f32x4*)(L + EX_OFF + ((wave >> 1) * 4 + 2 * (G & 1) + (G >> 1)) * PLANE + (ln & 15) * 16) = f32x4{hv0.x, hv0.y, hv1.x, hv1.y};
        else *(LAS v2u*)(L + KP_OFF + ((wave >> 1) * 2 + (G & 1)) * PLANE + (ln & 15) * 16 + 8 * (G >> 1)) = v2u{pk(hv0), pk(hv1)};
        sig<SY_KV>(vsw, 2 * pass + 1);
        SBAR(); op(IC<110>{}); SBAR();
#pragma unroll
        for (int g = 0; g < 4; ++g) *(LAS f32x4*)(vw + ((g >> 1) * 4 + (g & 1)) * PLANE) = f32x4{vv[2 * g].x, vv[2 * g].y, vv[2 * g + 1].x, vv[2 * g + 1].y};
        sig<SY_KV>(vsw, 2 * pass + 2);
        SBAR(); op(IC<111>{});
#pragma unroll
        for (int i = 8; i < 12; ++i) cs[i] = *(LAS f32x4*)(csb + 8 * (i - 8) * 4);
        res = ld_resid(rs, ln, wave, t, j, xin);
        SBAR();
      }
      CST(1, 2);
      spin_all_ge<SY_KV>(vs4, 2 * pass + 1);
      CST(1, 3);
      LAS char* kb = L + KP_OFF + h * 2 * PLANE + (16 + 32 * wave + tok - 14) * 16;
      LAS char* vb = L + EX_OFF + h * 4 * PLANE + (16 + 32 * wave + tok - 14) * 16;
      float lg[8]; v4u kk[16]; f32x4 vr[32];
#pragma unroll
      for (int w = 0; w < 8; ++w) { kk[2 * w] = *(LAS v4u*)(kb + (14 - 2 * w) * 16); kk[2 * w + 1] = *(LAS v4u*)(kb + PLANE + (14 - 2 * w) * 16); }
      SBAR();
      unsigned qq[8];
      {
        unsigned pq[8];
        sfor<0, 8>([&](auto I) { constexpr int i = I.v; pq[i] = pk(fma2(f32x2{cs[8 + (i >> 1)][2 * (i & 1)], cs[8 + (i >> 1)][2 * (i & 1) + 1]}, nmean, pr<i>(acc[0]))); });
#pragma unroll
        for (int i = 0; i < 4; ++i) { auto r = __builtin_amdgcn_permlane32_swap(pq[i], pq[4 + i], false, false); qq[i] = r[0]; qq[4 + i] = r[1]; }
      }
      pf = peek(vs4, SY_KV);
      SBAR();
      CST(1, 4);
      need_all<SY_KV>(pf, vs4, 2 * pass + 2);
      sfor<0, 4>([&](auto W) {
        constexpr int w = 2 * W.v;
#pragma unroll
        for (int g = 0; g < 8; ++g) vr[4 * w + g] = *(LAS f32x4*)(vb + (g & 3) * PLANE + (14 - 2 * (w + (g >> 2))) * 16);
        float a = dot2z(qq[0], kk[2 * w].x), b = dot2z(qq[0], kk[2 * w + 2].x);
#pragma unroll
        for (int i = 1; i < 8; ++i) { a = dot2(qq[i], kk[2 * w + (i >> 2)][i & 3], a); b = dot2(qq[i], kk[2 * w + 2 + (i >> 2)][i & 3], b); }
        lg[w] = a; lg[w + 1] = b;
        SBAR();
      });
      asm volatile("" : "+v"(lg[0]), "+v"(lg[1]), "+v"(lg[2]), "+v"(lg[3]), "+v"(lg[4]), "+v"(lg[5]), "+v"(lg[6]), "+v"(lg[7]));
      sigq(vq, pb + P_W2);
      hold<1>(vs, eb + 1);
      CST(1, 6);
      if (t == 0 && wave == 0) {
#pragma unroll
        for (int w = 1; w < 8; ++w) lg[w] = tok >= 2 * w ? lg[w] : -1e30f;
      }
      const float mx = max3(max3(lg[0], lg[1], lg[2]), max3(lg[3], lg[4], lg[5]), max3(lg[6], lg[7], lg[7]));
      const float sc = lns.rstd * (0.25f * 1.4426950408889634f);
      f32x2 e2[4];
#pragma unroll
      for (int w = 0; w < 4; ++w) { const f32x2 z = (f32x2{lg[2 * w], lg[2 * w + 1]} - sp(mx)) * sp(sc); e2[w] = f32x2{__builtin_amdgcn_exp2f(z.x), __builtin_amdgcn_exp2f(z.y)}; }
      const f32x2 s2 = (e2[0] + e2[1]) + (e2[2] + e2[3]);
      const f32x2 inv = sp(__builtin_amdgcn_rcpf(s2.x + s2.y));
      f32x2 ov[8];
#pragma unroll
      for (int w = 0; w < 8; ++w) {
        const f32x2 pw = sp(e2[w >> 1][w & 1]);
#pragma unroll
        for (int g = 0; g < 4; ++g) {
          const f32x4 v = vr[4 * w + g];
          ov[2 * g] = w ? fma2(f32x2{v[0], v[1]}, pw, ov[2 * g]) : f32x2{v[0], v[1]} * pw;
          ov[2 * g + 1] = w ? fma2(f32x2{v[2], v[3]}, pw, ov[2 * g + 1]) : f32x2{v[2], v[3]} * pw;
        }
      }
      const int oo = O_OFF + pass * X_SZ + (tb * 32 + 2 * j) * 1024 + h * 1024 + tok * 16;
      stx(v4u{pk(ov[0] * inv), pk(ov[1] * inv), pk(ov[2] * inv), pk(ov[3] * inv)}, rs, oo);
      stx(v4u{pk(ov[4] * inv), pk(ov[5] * inv), pk(ov[6] * inv), pk(ov[7] * inv)}, rs, oo + 512);
      CST(1, 7); CDUMP(1); CDUMP(5);
      STAMP(18 + 4 * ph);
      publish(vsw, eb + 1);
      STAMP(19 + 4 * ph);
    }
    { int ln = lane; asm volatile("" : "+v"(ln));
      proj_phase<16, false>(p, L, vs, rs, ln, wave, t, j, pass, have, O_OFF + pass * X_SZ, res, xin + X_SZ, stin + ST_SZ, eb + 1, ph + 1); }
    {
      int ln = lane; asm volatile("" : "+v"(ln));
      const int h = ln >> 5, tok = ln & 31;
      CDECL();
      v4u xf[32]; LnSteps lns;
      const int xb = opq(xin + X_SZ + tb * 32768), sb = opq(stin + ST_SZ + tb * 4096);
      const LB b3(L + ln * 16);
      f32x16 acc[6];
#pragma unroll
      for (int c = 0; c < 6; ++c)
#pragma unroll
        for (int i = 0; i < 16; ++i) acc[c][i] = 0.f;
      constexpr int PF = PFD;
      s16x8 wf[PF];
      auto rd = [&](auto N) -> s16x8 { return *(LAS s16x8*)b3.template at<slot_of(P_W3 + N.v)>(); };
      const unsigned vq = ln == 0 ? vsw + (SY_CONS - SY_OFF) : vs + (SY_DUMMY - SY_OFF) + 4 * ln;
      wait_landed(vs, have, pb + pos3(4));
      sfor<0, PF>([&](auto N) { wf[N.v] = rd(N); });
      SBAR();
      spin_ge<SY_READY>(vs, eb + 2);
      v2u pl; unsigned pc;
      STAMP(16 + 4 * (ph + 2));
      CST(3, 0);
      sfor<0, XW>([&](auto S) { xf[S.v] = ldf<S.v>(rs, ln * 16, xb); });
      SBAR();
      sfor<0, 192>([&](auto N) {
        constexpr int n = N.v;
        const s16x8 cur = wf[n % PF];
        if constexpr (n + PF < 192) {
          if constexpr (((n + PF) % 12 == 2 && n + PF > 12) || (n + PF) == 188) sigq(vq, pb + pos3((n + PF) / 6));
          if constexpr (n == 100) pc = peek(vs, SY_CS);
          if constexpr (n == 106) need_all<SY_CS>(pc, vs, 2u);
          if constexpr (!(AB & 8) && (n + PF) % 24 == 14 && n + PF < 24 * 7) pl = peek_landed(vs);
          if constexpr (!(AB & 8) && (n + PF) % 24 == 20 && n + PF < 24 * 7) take_landed(have, pl);
          if constexpr (!(AB & 8) && (n + PF) % 24 == 22 && n + PF < 24 * 7) wait_landed(vs, have, pb + pos3((n + PF) / 6 + 5));
          wf[n % PF] = rd(IC<n + PF>{});
        }
        acc[n % 6] = mfma32(cur, xf[n / 6], acc[n % 6]);
        {
          constexpr int st_ = n / 6, k = n % 6 == 0 ? XW + st_ + (st_ < E3 ? st_ : E3) : (n % 6 == 4 && st_ < E3 ? XW + 2 * st_ + 1 : 32);
          if constexpr (k < 32) { if constexpr (AB & 1) xf[k] = xf[k % 8]; else xf[k] = ldf<k>(rs, ln * 16, xb); }
        }
        if constexpr (!(AB & 2) && n % 6 == 2 && n / 6 < 8) lns.st[n / 6] = ldx2(rs, h * 2048 + tok * 8 + (n / 6) * 256, sb);
        if constexpr (!(AB & 2) && n >= 90 && n % 3 == 0 && (n - 90) / 3 < LnSteps::N) lns.template step<(n - 90) / 3>();
        if constexpr (n == 5) CST(3, 1); if constexpr (n == 47) CST(3, 2); if constexpr (n == 95) CST(3, 3); if constexpr (n == 119) CST(3, 4); if constexpr (n == 143) CST(3, 5); if constexpr (n == 167) CST(3, 6); if constexpr (n == 191) CST(3, 7);
        SBAR();
      });
      asm volatile("" : "+v"(acc[0]), "+v"(acc[1]), "+v"(acc[2]), "+v"(acc[3]), "+v"(acc[4]), "+v"(acc[5]));
      sig<SY_CONS>(vsw, pb + P_W4);
      CDUMP(3);
      STAMP(17 + 4 * (ph + 2));
      const f32x2 k1 = sp(lns.rstd * -2.3022081985f), k3 = sp(lns.rstd * lns.rstd * lns.rstd * -0.1029432397f), sd2 = sp(lns.sd * lns.sd), nmean = sp(lns.nmean);
      const int us = (j / 3) * 16 + (j % 3) * 6, nu = (j % 3 == 2) ? 4 : 6;
      const int ho = H_OFF + pass * H_SZ + (tb * 86 + us) * 1024 + ln * 16;
#pragma unroll
      for (int u = 0; u < 6; ++u) {
        if (u < 4 || nu == 6) {
          unsigned hv[4];
#pragma unroll
          for (int g = 0; g < 2; ++g) {
            const f32x4 cg = *(LAS f32x4*)(L + CS3_OFF + (32 * u + 8 * g + 4 * h) * 4), cu = *(LAS f32x4*)(L + CS3_OFF + (32 * u + 16 + 8 * g + 4 * h) * 4);
#pragma unroll
            for (int i = 0; i < 2; ++i) {
              const f32x2 tg = fma2(f32x2{cg[2 * i], cg[2 * i + 1]}, nmean, f32x2{acc[u][4 * g + 2 * i], acc[u][4 * g + 2 * i + 1]});
              const f32x2 tu = fma2(f32x2{cu[2 * i], cu[2 * i + 1]}, nmean, f32x2{acc[u][8 + 4 * g + 2 * i], acc[u][8 + 4 * g + 2 * i + 1]});
              const f32x2 z = tg * fma2(tg * tg, k3, k1);
              const f32x2 d = fma2(f32x2{__builtin_amdgcn_exp2f(z.x), __builtin_amdgcn_exp2f(z.y)}, sd2, sd2);
              hv[2 * g + i] = pk(tg * tu * f32x2{__builtin_amdgcn_rcpf(d.x), __builtin_amdgcn_rcpf(d.y)});
            }
          }
          stx(v4u{hv[0], hv[1], hv[2], hv[3]}, rs, ho + u * 1024);
          if (u == 3) { asm volatile("s_waitcnt vmcnt(1)" ::: "memory"); sig<SY_PROG>(vsw, eb + 3); }
        }
        SBAR();
      }
      res = ld_resid(rs, ln, wave, t, j, xin + X_SZ);
      STAMP(18 + 4 * (ph + 2));
      hold<3>(vs, eb + 4);
      publish(vsw, eb + 4);
      STAMP(19 + 4 * (ph + 2));
    }
    { int ln = lane; asm volatile("" : "+v"(ln));
      proj_phase<43, true>(p, L, vs, rs, ln, wave, t, j, pass, have, H_OFF + pass * H_SZ, res, xin + 2 * X_SZ, stin + 2 * ST_SZ, eb + 3, ph + 3); }
  }
}

__global__ void __launch_bounds__(512, 2) fused_kernel(Params p) {
  extern __shared__ __attribute__((aligned(16))) char smem[];
  LAS char* L = (LAS char*)smem;
  const int lane = threadIdx.x & 63, wave = __builtin_amdgcn_readfirstlane(threadIdx.x >> 6);
  const int b = blockIdx.x;
  const int t = ((b & 7) >> 1) * 4 + (b >> 6), j = (b & 1) * 8 + ((b >> 3) & 7);
  asm volatile(".subsection 2\n\t.fill 128, 4, 0xBF800000\n\t.subsection 0");
  if (threadIdx.x < 44) *(LAS unsigned*)(L + SY_OFF + threadIdx.x * 4) = 0u;
  __syncthreads();
  const unsigned lb = (unsigned)(size_t)L;
  unsigned vs = lb + SY_OFF; asm volatile("" : "+v"(vs));
  if (wave < 4) { __builtin_amdgcn_s_setprio(2); compute_main(p, L, vs, lane, wave, t, j); }
  else if (wave == 4) { __builtin_amdgcn_s_setprio(3); poller_main(p, L, vs, lane, t, j, wave); }
  else if (wave == 5) { __builtin_amdgcn_s_setprio(3); helper_main(p, L, lb, vs, lane, t, j); }
  else {
    loader_main(p, L, lb, lane, t, j, wave - 6, wave); }
  PROF_DUMP();
}
}

extern "C" void kernel_launch(void* const* d_in, const int* in_sizes, int n_in, void* d_out, int out_size, void* d_ws, size_t ws_size, hipStream_t stream) {
  Params p;
  p.x = (const u16*)d_in[0]; p.wqkv = (const u16*)d_in[1]; p.wo = (const u16*)d_in[2];
  p.wg = (const u16*)d_in[4]; p.wu = (const u16*)d_in[5]; p.wd = (const u16*)d_in[6];
  p.out = (u16*)d_out; p.ws = (unsigned char*)d_ws;
  (void)hipFuncSetAttribute((const void*)fused_kernel, hipFuncAttributeMaxDynamicSharedMemorySize, LDS_ALLOC);
  hipLaunchKernelGGL(fused_kernel, dim3(256), dim3(512), LDS_ALLOC, stream, p);
}
```

```cpp
#include <hip/hip_runtime.h>
#include <hip/hip_bf16.h>

#define GAS __attribute__((address_space(1)))
#define LAS __attribute__((address_space(3)))
#define DI __device__ __forceinline__
typedef unsigned v4u __attribute__((ext_vector_type(4)));
typedef unsigned v2u __attribute__((ext_vector_type(2)));
typedef float f32x2 __attribute__((ext_vector_type(2)));
typedef float f32x4 __attribute__((ext_vector_type(4)));
typedef float f32x16 __attribute__((ext_vector_type(16)));
typedef short s16x4 __attribute__((ext_vector_type(4)));
typedef short s16x8 __attribute__((ext_vector_type(8)));
typedef __bf16 bf16x8 __attribute__((ext_vector_type(8)));
typedef __bf16 bf16x2 __attribute__((ext_vector_type(2)));
typedef unsigned long long u64;
typedef unsigned short u16;
#define RLX_AGENT __ATOMIC_RELAXED, __HIP_MEMORY_SCOPE_AGENT

namespace {
constexpr int AUX_SC1 = 16;
constexpr int FLAG_OFF = 0;
constexpr int EPOCH_OFF = 65536;
constexpr int CSF_OFF = 131072;
constexpr int CSP_OFF = 262144;
constexpr int X_OFF = 1 << 20, X_SZ = 2 << 20;
constexpr int O_OFF = X_OFF + 4 * X_SZ;
constexpr int ST_OFF = O_OFF + 2 * X_SZ, ST_SZ = 64 * 16 * 256;
constexpr int H_OFF = ST_OFF + 4 * ST_SZ, H_SZ = 64 * 86 * 1024;
constexpr int WT1_OFF = H_OFF + 2 * H_SZ, WT3_OFF = WT1_OFF + 16 * 96 * 1024, WT2_OFF = WT3_OFF + 16 * 192 * 1024, WT4_OFF = WT2_OFF + 8 * 64 * 1024;
constexpr int WS_BYTES = WT4_OFF + 8 * 172 * 1024;
constexpr int PROF_OFF = 32 << 20;
static_assert(WS_BYTES <= PROF_OFF, "");
constexpr int NR = 136;
constexpr int EX_OFF = NR * 1024, PLANE = 144 * 16;
constexpr int CS1_OFF = EX_OFF + 8 * PLANE, CS3_OFF = CS1_OFF + 96 * 4;
constexpr int LN_OFF = CS3_OFF + 192 * 4;
constexpr int KP_OFF = 79 * 1024;
constexpr int SY_OFF = LN_OFF + 16 * 8;
constexpr int SY_LANDED = SY_OFF;
constexpr int SY_CONS = SY_OFF + 16;
constexpr int SY_WT = SY_OFF + 32, SY_RAW = SY_OFF + 144;
constexpr int SY_HOLD = SY_OFF + 40, SY_READY = SY_OFF + 44;
constexpr int SY_PROG = SY_OFF + 48;
constexpr int SY_KV = SY_OFF + 64;
constexpr int SY_XF = SY_OFF + 80;
constexpr int SY_CS = SY_OFF + 96;
constexpr int SY_T4 = SY_OFF + 104;
constexpr int SY_LN = SY_OFF + 112, SY_T2 = SY_OFF + 116, SY_SR = SY_OFF + 160;
constexpr int SY_TC = SY_OFF + 128;
constexpr int SY_DUMMY = SY_OFF + 176;
constexpr int LDS_BYTES = SY_DUMMY + 256;
constexpr int P_W1 = 0, P_W2 = 96, P_W3 = 160, P_W4 = 352, P_END = 544;
constexpr int QA = 24;
constexpr int pos1(int s, int c  ) { return s < QA ? 3 * s + c : (c < 2 ? 3 * QA + 2 * (s - QA) + c : 64 + s); }
constexpr int pos2(int a) { return P_W2 + 4 * a; }
constexpr int pos3(int s) { return P_W3 + 6 * s; }
constexpr int pos4(int a) { return P_W4 + 4 * a; }
constexpr int T4 = 34;
constexpr int ustart(int j) { return (j / 3) * 16 + (j % 3) * 6; }
constexpr bool early_unit(int kh, int u) { for (int i = 0; i < 8; ++i) if (u >= ustart(8 * kh + i) && u < ustart(8 * kh + i) + 3) return true; return false; }
constexpr int ord4(int kh, int a) {
  if (a < 24) return ustart(8 * kh + a / 3) + a % 3;
  int n = 24;
  for (int u = 43 * kh; u < 43 * kh + 43; ++u) if (!early_unit(kh, u)) { if (n == a) return u; ++n; }
  return -1;
}
static_assert(ord4(0, 42) == 42 && ord4(1, 24) == 43 && ord4(1, 42) == 85, "unit order");
constexpr int slot_of(int p) { return (p % NR) * 1024; }

struct Params { const u16* x; const u16* wqkv; const u16* wo; const u16* wg; const u16* wu; const u16* wd; u16* out; unsigned char* ws; };

DI unsigned rfl(unsigned v) { return __builtin_amdgcn_readfirstlane(v); }
DI u64 uni64(u64 e) { return ((u64)rfl((unsigned)(e >> 32)) << 32) | rfl((unsigned)e); }
DI float asf(unsigned u) { return __builtin_bit_cast(float, u); }
DI unsigned asu(float f) { return __builtin_bit_cast(unsigned, f); }
DI unsigned pk(float a, float b) { bf16x2 v = {(__bf16)a, (__bf16)b}; return __builtin_bit_cast(unsigned, v); }
DI float bflo(unsigned u) { return asf(u << 16); }
DI float bfhi(unsigned u) { return asf(u & 0xffff0000u); }
DI float rbf(float a) { return bflo(pk(a, a)); }
DI float dot2(unsigned a, unsigned b, float c) { return __builtin_amdgcn_fdot2_f32_bf16(__builtin_bit_cast(bf16x2, a), __builtin_bit_cast(bf16x2, b), c, false); }
DI float dot2z(unsigned a, unsigned b) { float r; asm("v_dot2_f32_bf16 %0, %1, %2, 0" : "=v"(r) : "v"(a), "v"(b)); return r; }
DI float max3(float a, float b, float c) { float r; asm("v_max3_f32 %0, %1, %2, %3" : "=v"(r) : "v"(a), "v"(b), "v"(c)); return r; }
DI float add1(float a, float b) { float r; asm("v_add_f32 %0, %1, %2" : "=v"(r) : "v"(a), "v"(b)); return r; }
DI float add3(float a, float b, float c) { return __builtin_constant_p(c) ? add1(a, b) : add1(add1(a, b), c); }
DI f32x2 fma2(f32x2 a, f32x2 b, f32x2 c) { return __builtin_elementwise_fma(a, b, c); }
DI f32x2 sp(float a) { return f32x2{a, a}; }
DI unsigned pk(f32x2 v) { return pk(v.x, v.y); }
template <int I> DI f32x2 pr(const f32x16& v) { return f32x2{v[2 * I], v[2 * I + 1]}; }
DI float sum32(float x) { auto r = __builtin_amdgcn_permlane32_swap(asu(x), asu(x), false, false); return asf(r[0]) + asf(r[1]); }
DI float sum16(float x) { auto r = __builtin_amdgcn_permlane16_swap(asu(x), asu(x), false, false); return asf(r[0]) + asf(r[1]); }
DI unsigned ror8(unsigned x) { return (unsigned)__builtin_amdgcn_update_dpp(0, (int)x, 0x128, 0xf, 0xf, false); }
DI f32x16 mfma32(s16x8 a, v4u b, f32x16 c) { return __builtin_amdgcn_mfma_f32_32x32x16_bf16(__builtin_bit_cast(bf16x8, a), __builtin_bit_cast(bf16x8, b), c, 0, 0, 0); }
DI f32x4 mfma16(s16x8 a, v4u b, f32x4 c) { return __builtin_amdgcn_mfma_f32_16x16x32_bf16(__builtin_bit_cast(bf16x8, a), __builtin_bit_cast(bf16x8, b), c, 0, 0, 0); }
DI s16x4 trr(LAS char* p) { return __builtin_amdgcn_ds_read_tr16_b64_v4i16((LAS s16x4*)p); }
DI s16x8 cat(s16x4 a, s16x4 b) { return __builtin_shufflevector(a, b, 0, 1, 2, 3, 4, 5, 6, 7); }
#define STR2(x) #x
#define STR(x) STR2(x)
#define PRE 16
#define MASKP 1
#define GAP 3
#define PRE1 24
#define PREA 16
#define PREB 16
#define PREW 24
#define GAPW 6
#define SLPS 8
#define SLPP 1
#define SLPC 1
#define SLPL 3
#define CBAR() asm volatile("" ::: "memory")
#define SBAR() __builtin_amdgcn_sched_barrier(0)
#define DRAIN() asm volatile("s_waitcnt vmcnt(0)" ::: "memory")

#define STAMP(i) do {} while (0)
#define PROF_DUMP() do {} while (0)
constexpr int LDS_ALLOC = LDS_BYTES;

#define CDECL() do {} while (0)
#define CST(site, i) do {} while (0)
#define CDUMP(site) do {} while (0)
template <int I> struct IC { static constexpr int v = I; };
template <int B, int E, class F> DI void sfor(F&& f) { if constexpr (B < E) { f(IC<B>{}); sfor<B + 1, E>(f); } }
DI int opq(int x) { asm volatile("" : "+s"(x)); return x; }
DI LAS char* opqv(LAS char* x) { asm volatile("" : "+v"(x)); return x; }

template <int OFF, int SLP = 0> DI void spin_ge(unsigned vs, unsigned need) {
  unsigned tv, ts, cnt;
  asm volatile("s_mov_b32 %2, 0x40000\n.Lsp%=:\n\tds_read_b32 %0, %3 offset:%5\n\ts_waitcnt lgkmcnt(0)\n\tv_readfirstlane_b32 %1, %0\n\t"
               "s_cmp_ge_u32 %1, %4\n\ts_cbranch_scc1 .Lsd%=\n\t.if %6\n\ts_sleep %6\n\t.endif\n\ts_sub_u32 %2, %2, 1\n\ts_cbranch_scc0 .Lsp%=\n.Lsd%=:"
               : "=&v"(tv), "=&s"(ts), "=&s"(cnt) : "v"(vs), "s"(need), "i"(OFF - SY_OFF), "i"(SLP) : "memory", "scc");
}
template <int OFF, int SLP = 0> DI void spin_all_ge(unsigned vs4, unsigned need) {
  unsigned tv, cnt;
  asm volatile("s_mov_b32 %1, 0x40000\n.Lsp%=:\n\tds_read_b32 %0, %2 offset:%4\n\ts_waitcnt lgkmcnt(0)\n\tv_cmp_gt_u32 vcc, %3, %0\n\t"
               "s_cbranch_vccz .Lsd%=\n\t.if %5\n\ts_sleep %5\n\t.endif\n\ts_sub_u32 %1, %1, 1\n\ts_cbranch_scc0 .Lsp%=\n.Lsd%=:"
               : "=&v"(tv), "=&s"(cnt) : "v"(vs4), "s"(need), "i"(OFF - SY_OFF), "i"(SLP) : "memory", "scc", "vcc");
}
DI void wait_landed(unsigned vs, unsigned& have, unsigned need) {
  unsigned t0, t1, cnt;
  asm volatile("s_cmp_ge_u32 %0, %5\n\ts_cbranch_scc0 .Lslow%=\n.Lback%=:\n\t.subsection 1\n.Lslow%=:\n\ts_mov_b32 %3, 0x40000\n.Lsp%=:\n\t"
               "ds_read_b32 %1, %4 offset:%6\n\tds_read_b32 %2, %4 offset:%6+4\n\ts_waitcnt lgkmcnt(0)\n\tv_min_u32 %1, %1, %2\n\ts_nop 0\n\tv_readfirstlane_b32 %0, %1\n\t"
               "s_cmp_ge_u32 %0, %5\n\ts_cbranch_scc1 .Lback%=\n\ts_sub_u32 %3, %3, 1\n\ts_cbranch_scc0 .Lsp%=\n\ts_branch .Lback%=\n\t.subsection 0"
               : "+s"(have), "=&v"(t0), "=&v"(t1), "=&s"(cnt) : "v"(vs), "s"(need), "i"(SY_LANDED - SY_OFF) : "memory", "scc");
}
template <int OFF> DI void sig(unsigned vs, unsigned val) {
  asm volatile("s_mov_b64 exec, 1\n\tds_write_b32 %0, %1 offset:%2\n\ts_mov_b64 exec, -1" :: "v"(vs), "v"(val), "i"(OFF - SY_OFF) : "memory");
}
DI void sigq(unsigned vq, unsigned val) { asm volatile("ds_write_b32 %0, %1" :: "v"(vq), "v"(val) : "memory"); }

DI unsigned peek(unsigned addr, int off) { return *(volatile LAS unsigned*)(size_t)(addr + off - SY_OFF); }
template <int OFF> DI void need_all(unsigned seen, unsigned vs4, unsigned need) {
  unsigned tv, cnt;
  asm volatile("v_cmp_gt_u32 vcc, %3, %5\n\ts_cbranch_vccnz .Lslow%=\n.Lback%=:\n\t.subsection 1\n.Lslow%=:\n\ts_mov_b32 %1, 0x40000\n.Lsp%=:\n\t"
               "ds_read_b32 %0, %2 offset:%4\n\ts_waitcnt lgkmcnt(0)\n\tv_cmp_gt_u32 vcc, %3, %0\n\t"
               "s_cbranch_vccz .Lback%=\n\ts_sub_u32 %1, %1, 1\n\ts_cbranch_scc0 .Lsp%=\n\ts_branch .Lback%=\n\t.subsection 0"
               : "=&v"(tv), "=&s"(cnt) : "v"(vs4), "s"(need), "i"(OFF - SY_OFF), "v"(seen) : "memory", "scc", "vcc");
}
DI v2u peek_landed(unsigned vs) { return *(volatile LAS v2u*)(size_t)(vs + SY_LANDED - SY_OFF); }
DI void take_landed(unsigned& have, v2u v) { asm volatile("s_max_u32 %0, %0, %1" : "+s"(have) : "s"(rfl(min(v.x, v.y))) : "scc"); }
DI void publish(unsigned vsw, unsigned ev) { DRAIN(); sig<SY_PROG>(vsw, ev); }

DI v4u ln_stats(const v4u (&st)[4]) {
  f32x2 s = {0.f, 0.f};
#pragma unroll
  for (int i = 0; i < 4; ++i) s += f32x2{asf(st[i].x), asf(st[i].z)};
  const f32x2 mean = f32x2{sum32(sum16(s.x)), sum32(sum16(s.y))} * sp(1.f / 512.f);
  f32x2 m2 = {0.f, 0.f};
#pragma unroll
  for (int i = 0; i < 4; ++i) { const f32x2 d = fma2(f32x2{asf(st[i].x), asf(st[i].z)}, sp(1.f / 32.f), -mean); m2 += fma2(d * sp(32.f), d, f32x2{asf(st[i].y), asf(st[i].w)}); }
  const float v0 = sum32(sum16(m2.x)) * (1.f / 512.f), v1 = sum32(sum16(m2.y)) * (1.f / 512.f);
  const float sd0 = rbf(__builtin_amdgcn_sqrtf(rbf(rbf(v0) + 1e-5f))), sd1 = rbf(__builtin_amdgcn_sqrtf(rbf(rbf(v1) + 1e-5f)));
  return v4u{asu(__builtin_amdgcn_rcpf(sd0)), asu(-rbf(mean.x)), asu(__builtin_amdgcn_rcpf(sd1)), asu(-rbf(mean.y))};
}
struct LnSteps {
  v2u st[8]; f32x2 a, b, c, d; float S, R, m16, rstd, nmean, sd;
  static constexpr int N = 22;
  DI f32x2 f(int i) const { return f32x2{asf(st[i].x), asf(st[i].y)}; }
  template <int I> DI void step() {
    if constexpr (I == 0) { a.x = add3(asf(st[0].x), asf(st[1].x), asf(st[2].x)); b.x = add3(asf(st[3].x), asf(st[4].x), asf(st[5].x)); }
    else if constexpr (I == 1) { a.y = add3(asf(st[0].y), asf(st[1].y), asf(st[2].y)); b.y = add3(asf(st[3].y), asf(st[4].y), asf(st[5].y)); }
    else if constexpr (I == 2) { a.x = add3(add3(a.x, asf(st[6].x), asf(st[7].x)), b.x, 0.f); }
    else if constexpr (I == 3) { a.y = add3(add3(a.y, asf(st[6].y), asf(st[7].y)), b.y, 0.f); }
    else if constexpr (I == 4) { S = sum32(a.x); }
    else if constexpr (I == 5) { m16 = S * (1.f / 16.f); nmean = S * (-1.f / 512.f); }
    else if constexpr (I == 6) { const float e = asf(st[0].x) - m16; R = e * e; }
    else if constexpr (I < 14) { const float e = asf(st[I - 6].x) - m16; R = __builtin_fmaf(e, e, R); }
    else if constexpr (I == 14) { R = __builtin_fmaf(R, 1.f / 32.f, a.y); }
    else if constexpr (I == 15) { R = sum32(R); }
    else if constexpr (I == 16) { R = rbf(R * (1.f / 512.f)); }
    else if constexpr (I == 17) { R = rbf(R + 1e-5f); }
    else if constexpr (I == 18) { R = __builtin_amdgcn_sqrtf(R); }
    else if constexpr (I == 19) { R = rbf(R); }
    else if constexpr (I == 20) { sd = R; rstd = __builtin_amdgcn_rcpf(R); }
    else if constexpr (I == 21) { nmean = rbf(nmean); }

    if constexpr (I == 4) asm volatile("" : "+v"(S)); if constexpr (I == 5) asm volatile("" : "+v"(m16), "+v"(nmean));
    if constexpr (I >= 6 && I < 20) asm volatile("" : "+v"(R)); if constexpr (I == 20) asm volatile("" : "+v"(rstd)); if constexpr (I == 21) asm volatile("" : "+v"(nmean));
  }
};
DI v4u ldx(__amdgpu_buffer_rsrc_t rs, int voff, int soff) { return __builtin_amdgcn_raw_buffer_load_b128(rs, voff, soff, AUX_SC1); }
DI v2u ldx2(__amdgpu_buffer_rsrc_t rs, int voff, int soff) { return __builtin_amdgcn_raw_buffer_load_b64(rs, voff, soff, AUX_SC1); }

#define MAPB 1
#define AB 0
#define DEPTH1 20
#define NE1 0
#define W1GATE SY_SWEPT
DI void stx(v4u v, __amdgpu_buffer_rsrc_t rs, int voff) { __builtin_amdgcn_raw_buffer_store_b128(v, rs, voff, 0, AUX_SC1); }
DI void stx2(v2u v, __amdgpu_buffer_rsrc_t rs, int voff) { __builtin_amdgcn_raw_buffer_store_b64(v, rs, voff, 0, AUX_SC1); }
#define CS3E 8
#define G2WORD SY_GO2
#define GATEN 60
#define CODEPF 1280
#define E1 2
#define E3 0
#define PFD 6
#define STAGED 1
#define XP 10
#define E4 0
#define XW 8
#define DEPTH 12
struct Loader {
  unsigned vsl  , vs6  , vs, cons; int pub;
#define LOOK "ds_read_b32 %3, %4 offset:%7\n\ts_waitcnt lgkmcnt(0)\n\t" \
             "v_min_u32_dpp %3, %3, %3 row_shr:2 row_mask:0xf bank_mask:0xf\n\ts_nop 1\n\t" \
             "v_min_u32_dpp %3, %3, %3 row_shr:1 row_mask:0xf bank_mask:0xf\n\ts_nop 0\n\t" \
             "v_readlane_b32 %0, %3, 3\n\ts_cmp_ge_u32 %0, %5\n\ts_cbranch_scc1 .Lback%=\n\t"
#define RUNG(N) "s_waitcnt vmcnt(" #N ")\n\ts_sub_i32 %1, %8, 2 * " #N "\n\ts_max_i32 %2, %2, %1\n\tv_mov_b32 %3, %2\n\tds_write_b32 %6, %3\n\t" LOOK
  unsigned tmp;
  DI void room(unsigned pos, unsigned end) {
    unsigned cnt;
    asm volatile("s_cmp_ge_i32 %0, %5\n\ts_cbranch_scc0 .Lslow%=\n.Lback%=:\n\t.subsection 1\n.Lslow%=:\n\t" LOOK RUNG(8) RUNG(4)
                 "s_waitcnt vmcnt(0)\n\tv_mov_b32 %3, %8\n\ts_mov_b32 %2, %8\n\tds_write_b32 %6, %3\n\ts_mov_b32 %1, 0x40000\n.Lsp%=:\n\t" LOOK
                 "s_sleep " STR(SLPL) "\n\ts_sub_u32 %1, %1, 1\n\ts_cbranch_scc0 .Lsp%=\n\ts_branch .Lback%=\n\t.subsection 0"
                 : "+s"(cons), "=&s"(cnt), "+s"(pub) : "v"(tmp), "v"(vs6), "s"(end - NR), "v"(vsl), "i"(SY_CONS - SY_OFF), "s"(pos) : "memory", "scc");
  }
  unsigned hv, rv;
  DI void look() { asm volatile("ds_read_b32 %0, %2 offset:%3\n\tds_read_b32 %1, %2 offset:%4" : "=&v"(hv), "=&v"(rv) : "v"(vs), "i"(SY_HOLD - SY_OFF), "i"(SY_READY - SY_OFF) : "memory"); }
  DI void done(unsigned end) {
    pub = max((int)end - 2 * DEPTH, pub);
    unsigned cnt;
    asm volatile("s_waitcnt vmcnt(%7) lgkmcnt(0)\n\tds_write_b32 %4, %5\n\tv_cmp_gt_u32 vcc, %2, %3\n\ts_cbranch_vccnz .Lslow%=\n.Lback%=:\n\t.subsection 1\n.Lslow%=:\n\t"
                 "s_waitcnt vmcnt(0)\n\tv_mov_b32 %2, %6\n\ts_mov_b32 %0, %6\n\tds_write_b32 %4, %2\n\ts_mov_b32 %1, 0x40000\n.Lsp%=:\n\t"
                 "ds_read_b32 %2, %8 offset:%9\n\tds_read_b32 %3, %8 offset:%10\n\ts_waitcnt lgkmcnt(0)\n\tv_cmp_gt_u32 vcc, %2, %3\n\ts_cbranch_vccz .Lback%=\n\t"
                 "s_sub_u32 %1, %1, 1\n\ts_cbranch_scc0 .Lsp%=\n\t"
                 "s_branch .Lback%=\n\t.subsection 0"
                 : "+s"(pub), "=&s"(cnt) : "v"(hv), "v"(rv), "v"(vsl), "v"(pub), "s"(end), "i"(DEPTH), "v"(vs), "i"(SY_HOLD - SY_OFF), "i"(SY_READY - SY_OFF) : "memory", "scc", "vcc");
  }
};
#define GL0(v, rs, so, c) "buffer_load_dwordx4 " v ", " rs ", " so " offen" c " lds\n\t"
#define GL1(v, rs, so, c) "s_nop 0\n\t" GL0(v, rs, so, c)
#define GLN(v, rs, so, c) "s_add_u32 m0, m0, 0x800\n\t" GL1(v, rs, so, c)
DI v4u srd(const void* base, unsigned bytes) { const u64 a = (u64)base; return v4u{(unsigned)a, (unsigned)(a >> 32) & 0xFFFFu, bytes, 0x00020000u}; }
DI void dma1(unsigned lds, unsigned voff, v4u r, unsigned so) { asm volatile("s_mov_b32 m0, %0\n\t" GL1("%1", "%2", "%3", "") :: "s"(lds), "v"(voff), "s"(r), "s"(so) : "memory"); }
DI int wg_of(int tt, int jj) { return ((tt & 3) * 8 + (jj & 7)) * 8 + (tt >> 2) * 2 + (jj >> 3); }
DI void frag_out(s16x8 f, __amdgpu_buffer_rsrc_t rs, int off, float& cs) {
  const v4u v = __builtin_bit_cast(v4u, f); const unsigned ones = 0x3f803f80u;
  stx(v, rs, off);
  cs = dot2(v.w, ones, dot2(v.z, ones, dot2(v.y, ones, dot2(v.x, ones, cs))));
}
constexpr int STG1 = 96 * 1024, STG2 = 104 * 1024, STG4 = 112 * 1024, STG3 = 120 * 1024;

DI void prep3(const Params& p, LAS char* L, unsigned lb, unsigned vs, __amdgpu_buffer_rsrc_t rs, int lane, int t, int j, int l) {
  const int h = lane >> 5, gi = (lane >> 4) & 1, q = (lane & 15) >> 2, pp = lane & 3, us = (j / 3) * 16 + (j % 3) * 6, ks = 2 * t + l;
  LAS char* bE = L + STG3 + l * 6144 + (4 * h + q) * 192 + 8 * pp + gi * (3072 + 32); LAS char* bO = bE - gi * 64;
  const v4u rg = srd(p.wg, 512 * 2752), ru = srd(p.wu, 512 * 2752);
  spin_all_ge<SY_RAW>(vs + 4 * (lane & 3), 1u);
#pragma unroll
  for (int i = 0; i < 6; ++i) {
    const int e = 64 * (i % 3) + lane, r = (e * 43691) >> 19, c = e - 12 * r, up = i / 3;
    dma1(lb + STG3 + (6 * l + i) * 1024, r * 2752 + (c ^ (2 * up)) * 16 + 32 * us, up ? ru : rg, ks * (16 * 2752));
  }
  DRAIN();
  s16x8 f[6];
#pragma unroll
  for (int u = 0; u < 6; ++u) { LAS char* b = ((u & 1) ? bO : bE) + 32 * u; f[u] = cat(trr(b), trr(b + 1536)); }
  sig<SY_SR>(vs + 4 * l, 1u);
  float cs[6];
#pragma unroll
  for (int u = 0; u < 6; ++u) { cs[u] = 0.f; frag_out(f[u], rs, WT3_OFF + (j * 192 + 6 * ks + u) * 1024 + lane * 16, cs[u]); }
#pragma unroll
  for (int u = 0; u < 6; u += 2) {
    auto r = __builtin_amdgcn_permlane32_swap(asu(cs[u]), asu(cs[u + 1]), false, false);
    __builtin_amdgcn_raw_buffer_store_b32(asu(asf(r[0]) + asf(r[1])), rs, CSP_OFF + ((j * 32 + ks) * 288 + 96 + 32 * u + lane) * 4, 0, AUX_SC1);
  }
  DRAIN();
}

DI void loader_main(const Params& p, LAS char* L, unsigned lb, int lane, int t, int j, int l, int wave) {
  const __amdgpu_buffer_rsrc_t rs = __builtin_amdgcn_make_buffer_rsrc((void*)p.ws, 0, WS_BYTES, 0x00020000);
  Loader ld; ld.vsl = lane == 0 ? lb + SY_LANDED + 4 * l : lb + SY_DUMMY + 4 * lane; ld.vs6 = lb + SY_OFF + 4 * (lane & 3);
  ld.cons = opq(0); ld.pub = opq(0); ld.vs = lb + SY_OFF; asm volatile("v_mov_b32 %0, 0" : "=v"(ld.tmp));
  auto fresh = [&]() { int x = lane; asm volatile("" : "+v"(x)); return x; };
  const int jc = j >> 1, idx = 2 * t + (j & 1);
  auto off2 = [&]() { const int x = fresh(); return (unsigned)((x >> 3) * 1024 + (((x & 7) ^ (((x >> 4) & 1) * 4)) * 16) + 128 * jc); };
  auto rd2 = [&](int stg, int c) {
    const int x = fresh(), h = x >> 5, gi = (x >> 4) & 1, q = (x & 15) >> 2, pp = x & 3;
    LAS char* b = L + stg + (((4 * h + q) * 128 + ((2 * gi + (pp >> 1)) ^ (4 * ((q >> 1) & 1))) * 16 + 8 * (pp & 1)) ^ (64 * c));
    return __builtin_bit_cast(v4u, cat(trr(b), trr(b + 1024)));
  };
  if (l == 0) {
    const v4u ro = srd(p.wo, 512 * 1024); const unsigned o2 = off2();
    dma1(lb + STG2, o2, ro, idx * 16384); dma1(lb + STG2 + 1024, o2 + 8192, ro, idx * 16384);
    DRAIN();
#pragma unroll
    for (int c = 0; c < 2; ++c) stx(rd2(STG2, c), rs, WT2_OFF + (jc * 64 + 4 * (idx & 15) + 2 * (idx >> 4) + c) * 1024 + lane * 16);
    DRAIN(); sig<SY_T2>(ld.vs, 1u);
  }
  const v4u rw = srd(p.ws, WS_BYTES);
  const unsigned lbl = lb + l * 1024;
  auto seg = [&](unsigned so, unsigned pos, int n) {
    const unsigned v0 = fresh() * 16 + l * 1024;
    unsigned m = lbl + (pos % NR) * 1024;
#pragma nounroll
    for (int i = 0; i < n; ++i) {
      ld.room(pos, pos + 8);
      ld.look();
      asm volatile("s_mov_b32 m0, %0\n\t" GL1("%1", "%5", "%6", " sc1") GLN("%2", "%5", "%6", " sc1") GLN("%3", "%5", "%6", " sc1") GLN("%4", "%5", "%6", " sc1")
                   :: "s"(m), "v"(v0), "v"(v0 + 2048), "v"(v0 + 4096), "v"(v0 + 6144), "s"(rw), "s"(so) : "memory", "scc");
      pos += 8; so += 8192; m += 8192; if (m >= lbl + NR * 1024) m -= NR * 1024;
      ld.done(pos);
    }
  };
  v4u w1[8][6];
  LAS char* L1; LAS char* L1b;
  auto base1 = [&]() { L1 = (LAS char*)(size_t)(lb + fresh() * 16 + l * 6144); L1b = opqv(L1 + 49152); };
  {
    spin_ge<SY_WT>(ld.vs, 1);
    STAMP(16);
    const unsigned v0 = fresh() * 16 + l * 1024;
    unsigned m = lbl, so = WT1_OFF + j * 96 * 1024;
#pragma nounroll
    for (int i = 0; i < 12; ++i) {
      asm volatile("s_mov_b32 m0, %0\n\t" GL1("%1", "%5", "%6", " sc1") GLN("%2", "%5", "%6", " sc1") GLN("%3", "%5", "%6", " sc1") GLN("%4", "%5", "%6", " sc1")
                   "s_waitcnt vmcnt(%7)\n\tds_write_b32 %8, %9"
                   :: "s"(m), "v"(v0), "v"(v0 + 2048), "v"(v0 + 4096), "v"(v0 + 6144), "s"(rw), "s"(so), "i"(DEPTH1), "v"(ld.vsl), "v"(max(8 * i + 8 - 2 * DEPTH1, 0)) : "memory", "scc");
      so += 8192; m += 8192;
    }
    STAMP(17);
  }
  {
    asm volatile("s_waitcnt vmcnt(12)" ::: "memory"); sigq(ld.vsl, 72);
    DRAIN(); sigq(ld.vsl, 73);
    STAMP(20);
    spin_all_ge<SY_LANDED>(ld.vs + 4 * (lane & 1), 73);
    base1();
    sfor<0, 8>([&](auto I) {
#pragma unroll
      for (int q = 0; q < 6; ++q) w1[I.v][q] = *(LAS v4u*)((I.v < 4 ? L1 : L1b) + (I.v & 3) * 12288 + q * 1024);
      SBAR();
    });
    ld.pub = opq(P_W2); sigq(ld.vsl, ld.pub);
    STAMP(21);
  }
  seg(WT2_OFF + jc * 64 * 1024, P_W2, 2);
  {
    const v4u rd = srd(p.wd, 1376 * 1024); const unsigned o2 = off2();
    spin_all_ge<SY_RAW>(ld.vs + 4 * (lane & 3), 1u);
#pragma unroll
    for (int i = 0; i < 4; ++i) if (i < 2 || l == 0) dma1(lb + STG4 + l * 4096 + i * 1024, o2 + (i & 1) * 8192, rd, (idx + 32 * (i < 2 ? l : 2)) * 16384);
    DRAIN(); ld.pub = opq(P_W2 + 16); sigq(ld.vsl, ld.pub);
    v4u f[4];
#pragma unroll
    for (int i = 0; i < 4; ++i) f[i] = rd2(STG4 + l * 4096 + (i >> 1) * 2048, i & 1);
    sig<SY_SR + 8>(ld.vs + 4 * l, 1u);
#pragma unroll
    for (int i = 0; i < 2; ++i) {
      const int u = idx + 32 * (i ? 2 : l), kh = u >= 43, r16 = u & 15, jp = 3 * (u >> 4) + (r16 >= 12 ? 2 : r16 >= 6), r = r16 - (r16 >= 12 ? 12 : (r16 >= 6 ? 6 : 0)), jr = jp - 8 * kh;
      const int a = (r < 3 && jr >= 0) ? 3 * jr + r : 24 + u - 43 * kh - 3 * (jr + 1);
      if (u < 86 && (i == 0 || l == 0)) {
#pragma unroll
        for (int c = 0; c < 2; ++c) stx(f[2 * i + c], rs, WT4_OFF + (jc * 172 + 4 * a + 2 * kh + c) * 1024 + lane * 16);
      }
    }
    DRAIN(); sig<SY_TC + 4>(ld.vs + 4 * l, 1u);
    STAMP(22);
    spin_all_ge<SY_SR>(ld.vs + 4 * (lane & 3), 1u);
  }
#pragma nounroll
  for (int pass = 0; pass < 2; ++pass) {
    const unsigned pb = pass * P_END;
    STAMP(40 + 8 * pass);
    seg(WT2_OFF + (jc * 64 + 16 - 16 * pass) * 1024, pb + P_W2 + 16 - 16 * pass, 6 + 2 * pass);
    STAMP(41 + 8 * pass);
    if (pass == 0) { DRAIN(); ld.pub = opq(P_W3); sigq(ld.vsl, ld.pub); spin_ge<SY_WT>(ld.vs, 2); }
    seg(WT3_OFF + j * 192 * 1024, pb + P_W3, 24);
    STAMP(42 + 8 * pass);
    seg(WT4_OFF + jc * 172 * 1024, pb + P_W4, T4 / 2);
    DRAIN(); ld.pub = opq(pb + P_END); sigq(ld.vsl, ld.pub);
    STAMP(43 + 8 * pass);
    if (pass == 1) break;
    base1();
    sfor<0, 8>([&](auto I) {
      const unsigned pos = P_END + 12 * I.v + 6 * l;
      ld.room(pos, pos + 6);
#pragma unroll
      for (int q = 0; q < 6; ++q) *(LAS v4u*)((I.v < 4 ? L1 : L1b) + (I.v & 3) * 12288 + q * 1024) = w1[I.v][q];
      ld.pub = opq(I.v < 7 ? pos + 12 : P_END + P_W2); sigq(ld.vsl, ld.pub);
    });
  }
}

struct Tail {
  v4u r[2 * (43 - T4)];
  DI void load(__amdgpu_buffer_rsrc_t rs, unsigned vs, int lane, int j, int e) {
    spin_ge<SY_WT>(vs, 2);
#pragma unroll
    for (int i = 0; i < 2 * (43 - T4); ++i) r[i] = ldx(rs, lane * 16 + (i & 1) * 1024, WT4_OFF + ((j >> 1) * 172 + 4 * (T4 + (i >> 1)) + 2 * e) * 1024);
    SBAR();
  }
  DI void put(LAS char* L, unsigned vs, int lane, int e, int pass) {
    spin_all_ge<SY_CONS, SLPS>(vs + 4 * (lane & 3), pass * P_END + pos4(43) - NR);
#pragma unroll
    for (int i = 0; i < 2 * (43 - T4); ++i) *(LAS v4u*)(L + slot_of(pos4(T4)) + (4 * (i >> 1) + (i & 1)) * 1024 + e * 2048 + lane * 16) = r[i];
    sig<SY_T4>(vs + 4 * e, pass + 1);
  }
};
DI void helper_main(const Params& p, LAS char* L, unsigned lb, unsigned vs, int lane, int t, int j) {
  const int wave = 5; (void)wave;
  const __amdgpu_buffer_rsrc_t rs = __builtin_amdgcn_make_buffer_rsrc((void*)p.ws, 0, WS_BYTES, 0x00020000);
  const int h = lane >> 5, gi = (lane >> 4) & 1, q = (lane & 15) >> 2, pp = lane & 3;
  {
    const v4u rq = srd(p.wqkv, 512 * 3072);
    const unsigned o1 = (lane >> 2) * 3072 + (lane & 3) * 16 + 64 * j;
#pragma unroll
    for (int i = 0; i < 6; ++i) dma1(lb + STG1 + i * 1024, o1 + (i % 3) * 1024, rq, (2 * t + i / 3) * (16 * 3072));
    const u64 e_ = __hip_atomic_load((GAS u64*)(p.ws + EPOCH_OFF), RLX_AGENT);
    asm volatile("s_waitcnt vmcnt(1)" ::: "memory");
    STAMP(12);
    LAS char* b1 = L + STG1 + (4 * h + q) * 64 + gi * 32 + pp * 8;
#pragma unroll
    for (int i = 0; i < 6; ++i) {
      const int ks = 2 * t + i / 3, c = (i % 3 + 2) % 3;
      const int pos = ks < QA ? 3 * ks + c : (c < 2 ? 3 * QA + 2 * (ks - QA) + c : 64 + ks);
      float cs = 0.f;
      frag_out(cat(trr(b1 + i * 1024), trr(b1 + i * 1024 + 512)), rs, WT1_OFF + (j * 96 + pos) * 1024 + lane * 16, cs);
      cs = sum32(cs);
      if (lane < 32) __builtin_amdgcn_raw_buffer_store_b32(asu(cs), rs, CSP_OFF + ((j * 32 + ks) * 288 + 32 * (i % 3) + lane) * 4, 0, AUX_SC1);
    }
    DRAIN();
    const u64 E = uni64(e_);
    GAS u64* mine = (GAS u64*)(p.ws + CSF_OFF + blockIdx.x * 256);
    spin_ge<SY_T2>(vs, 1u);
    if (lane == 0) __hip_atomic_store(mine, E + 1, RLX_AGENT);
    auto poll = [&](GAS u64* f, int n, u64 want) {
      __builtin_amdgcn_s_sleep(PREW);
      bool ok = lane >= n;
      for (unsigned k = 0; k < (1u << 16); ++k) {
        if (!ok) ok = __hip_atomic_load(f, RLX_AGENT) - want <= E + 2 - want;
        if (__all(ok)) break;
        if (GAPW) __builtin_amdgcn_s_sleep(GAPW);
      }
      __builtin_amdgcn_fence(__ATOMIC_ACQUIRE, "wavefront");
    };
    GAS u64* f = (GAS u64*)(p.ws + CSF_OFF + wg_of(lane & 15, j ^ ((lane >> 4) & 1)) * 256);
    poll(f, 32, E + 1);
    sig<SY_WT>(vs, 1u);
    STAMP(13);
    {
      const int l2 = lane < 48 ? lane : lane - 48, hi = l2 >= 24;
      v4u g[16];
#pragma unroll
      for (int i = 0; i < 16; ++i) g[i] = ldx(rs, hi * 1152 + (l2 - 24 * hi) * 16, CSP_OFF + (j * 32 + 2 * i) * 1152);
      SBAR();
      f32x4 tot = __builtin_bit_cast(f32x4, g[0]);
#pragma unroll
      for (int i = 1; i < 16; ++i) tot += __builtin_bit_cast(f32x4, g[i]);
#pragma unroll
      for (int i = 0; i < 4; ++i) tot[i] += asf(__builtin_amdgcn_ds_bpermute((lane + 24) * 4, asu(tot[i])));
      if (lane < 24) *(LAS f32x4*)(L + CS1_OFF + lane * 16) = tot;
      sig<SY_CS>(vs, 1u);
      STAMP(15);
    }
    prep3(p, L, lb, vs, rs, lane, t, j, 1);
    spin_all_ge<SY_TC>(vs + 4 * min(lane & 3, 2), 1u);
    STAMP(17);
    if (lane == 0) __hip_atomic_store(mine, E + 2, RLX_AGENT);
    poll(f, 32, E + 2);
    sig<SY_WT>(vs, 2u);
    STAMP(14);
    {
      f32x4 tot = {0.f, 0.f, 0.f, 0.f};
#pragma unroll
      for (int k = 0; k < 2; ++k) {
        v4u g[16];
#pragma unroll
        for (int i = 0; i < 16; ++i) g[i] = ldx(rs, 384 + min(lane, 47) * 16, CSP_OFF + (j * 32 + 16 * k + i) * 1152);
        SBAR();
#pragma unroll
        for (int i = 0; i < 16; ++i) tot += __builtin_bit_cast(f32x4, g[i]);
      }
      if (lane < 48) *(LAS f32x4*)(L + CS3_OFF + lane * 16) = tot;
      sig<SY_CS>(vs, 2u);
    }
  }
  spin_ge<SY_READY, SLPS>(vs, 3u);
  Tail tl; tl.load(rs, vs, lane, j, 1);
  tl.put(L, vs, lane, 1, 0);
  tl.put(L, vs, lane, 1, 1);
}

DI void poller_main(const Params& p, LAS char* L, unsigned vs, int lane, int t, int j, int wave) {
  const __amdgpu_buffer_rsrc_t rs = __builtin_amdgcn_make_buffer_rsrc((void*)p.ws, 0, WS_BYTES, 0x00020000);
  const u64 E = uni64(__hip_atomic_load((GAS u64*)(p.ws + EPOCH_OFF), RLX_AGENT));
  const int own = wg_of(t, lane & 15), prev = wg_of(t > 0 ? t - 1 : 0, lane & 15);
  auto hop = [&](unsigned ev, bool halo) {
    spin_all_ge<SY_PROG, SLPP>(vs + 4 * (lane & 3), ev);
    if (lane == 0) __hip_atomic_store((GAS u64*)(p.ws + FLAG_OFF + blockIdx.x * 256), E + ev, RLX_AGENT);
    GAS u64* f = (GAS u64*)(p.ws + FLAG_OFF + ((halo && lane >= 16) ? prev : own) * 256);
    if (ev == 1) __builtin_amdgcn_s_sleep(PRE1); else if (ev % 5 == 4) __builtin_amdgcn_s_sleep(PREA); else if (ev % 5 == 0) __builtin_amdgcn_s_sleep(PREB); else
    __builtin_amdgcn_s_sleep(PRE);
    bool ok = lane >= (halo ? 32 : 16);
    for (unsigned n = 0; n < (1u << 16); ++n) {
      if (!ok || !MASKP) ok = lane >= (halo ? 32 : 16) || __hip_atomic_load(f, RLX_AGENT) - (E + ev) <= (u64)(10u - ev);
      if (__all(ok)) break;
      if (GAP) __builtin_amdgcn_s_sleep(GAP);
    }
    __builtin_amdgcn_fence(__ATOMIC_ACQUIRE, "wavefront");
    sig<SY_READY>(vs, ev);
  };
  Tail tl;
  sfor<0, 2>([&](auto PASS) {
    constexpr int pass = PASS.v;
    hop(1 + 5 * pass, true);
    STAMP(1 + 4 * pass);
    {
      const int hb = t > 0 ? 4 * t - 1 : 0, G = lane >> 4;
      const int hvo = (G >> 1) * 1024 + (16 + (lane & 15) + 32 * (G & 1)) * 16, xb = X_OFF + 2 * pass * X_SZ + hb * 32768;
      v4u a[4], hx[16];
#pragma unroll
      for (int i = 0; i < 4; ++i) a[i] = ldx(rs, lane * 16 + i * 1024, ST_OFF + 2 * pass * ST_SZ + hb * 4096);
#pragma unroll
      for (int i = 0; i < 16; ++i) hx[i] = ldx(rs, hvo + (i & 1) * 2048, xb + (i >> 1) * 4096);
      SBAR();
      const v4u r = ln_stats(a);
      if (lane >= 8 && lane < 16) *(LAS v4u*)(L + LN_OFF + (2 * lane - 16) * 8) = r;
#pragma unroll
      for (int i = 0; i < ((AB & 4) ? 1 : 16); ++i) *(LAS v4u*)(L + EX_OFF + i * 1024 + lane * 16) = hx[i];
      sig<SY_LN>(vs, pass + 1);
    }
    if constexpr (pass == 0) { prep3(p, L, (unsigned)(size_t)L, vs, rs, lane, t, j, 0); sig<SY_TC>(vs, 1u); }
#pragma nounroll
    for (unsigned e = 2; e <= 5; ++e) {
      hop(e + 5 * pass, false);
      if (e != 5) STAMP(e + 4 * pass); else STAMP(62 + pass);
      if (pass == 0 && e == 3) tl.load(rs, vs, lane, j, 0);
    }
    tl.put(L, vs, lane, 0, pass);
  });
  if (blockIdx.x == 0) {
    for (unsigned n = 0; n < (1u << 16); ++n) {
      bool ok = true;
#pragma unroll
      for (int i = 0; i < 4; ++i) ok &= (__hip_atomic_load((GAS u64*)(p.ws + FLAG_OFF + (i * 64 + lane) * 256), RLX_AGENT) - (E + 2) <= 8ull);
      if (__all(ok)) break;
    }
    if (lane == 0) __hip_atomic_store((GAS u64*)(p.ws + EPOCH_OFF), E + 16, RLX_AGENT);
  }
}

template <int S> DI v4u ldf(__amdgpu_buffer_rsrc_t rs, int voff, int base) { return ldx(rs, voff + (S & 3) * 1024, base + (S >> 2) * 4096); }
struct LB { LAS char* b[3]; DI explicit LB(LAS char* x) { b[0] = opqv(x); b[1] = opqv(x + 60 * 1024); b[2] = opqv(x + 120 * 1024); }
  template <int OFF> DI LAS char* at() const { return b[OFF / (60 * 1024)] + OFF % (60 * 1024); } };

#define HOLDMASK 0
template <int PH> DI void hold(unsigned vs, unsigned ev) { if constexpr ((HOLDMASK >> PH) & 1) sig<SY_HOLD>(vs, ev); }
struct Resid { v4u a, b; };
DI Resid ld_resid(__amdgpu_buffer_rsrc_t rs, int lane, int wave, int t, int j, int r_off) {
  const int ro = opq(r_off + ((4 * t + 2 * (j & 1) + (wave & 1)) * 32 + 4 * (j >> 1) + 2 * (wave >> 1)) * 1024);
  return Resid{ldf<0>(rs, lane * 16, ro), ldf<1>(rs, lane * 16, ro)};
}
template <int NKS, bool IS_DOWN>
DI void proj_phase(const Params& p, LAS char* L, unsigned vs, __amdgpu_buffer_rsrc_t rs, int lane, int wave, int t, int j, int pass, unsigned& have,
                   int a_off  , const Resid& res, int x_off, int st_off, unsigned ev  , unsigned ph) {
  const int m2 = wave & 1, kh = wave >> 1, th = j & 1, jc = j >> 1, tb = 4 * t + 2 * th + m2;
  const int h = lane >> 5;
  const unsigned pb = pass * P_END, vsw = vs + 4 * wave;
  const unsigned vq = lane == 0 ? vsw + (SY_CONS - SY_OFF) : vs + (SY_DUMMY - SY_OFF) + 4 * lane;
  f32x16 acc[2];
#pragma unroll
  for (int i = 0; i < 4; ++i) { acc[0][2 * i] = bflo(res.a[i]); acc[0][2 * i + 1] = bfhi(res.a[i]); acc[0][8 + 2 * i] = bflo(res.b[i]); acc[0][9 + 2 * i] = bfhi(res.b[i]); }
#pragma unroll
  for (int i = 0; i < 16; ++i) acc[1][i] = 0.f;
  asm volatile("" : "+v"(acc[0]));
  auto posk = [](int a) { return IS_DOWN ? pos4(a) : pos2(a); };
  const LB b0(L + lane * 16 + kh * 3072), b1(L + lane * 16 + kh * 1024 + 1024);
  auto rd = [&](auto N) -> s16x8 {
    constexpr int a = N.v >> 1, c = N.v & 1, sl = slot_of(IS_DOWN ? pos4(a) : pos2(a));
    const LB& b = c ? b1 : b0;
    return *(LAS s16x8*)b.template at<sl>();
  };
  constexpr int NOP = 2 * NKS, PF = PFD, LAST = IS_DOWN ? T4 : NKS;
  s16x8 wf[PF];
  wait_landed(vs, have, pb + posk(8));
  sfor<0, PF>([&](auto N) { wf[N.v] = rd(N); });
  const int ab = opq(a_off + tb * (IS_DOWN ? 86 : 32) * 1024 + (IS_DOWN ? 0 : kh * 16384)), khk = opq(kh * 1024);
  v4u xf[NKS];
  auto ldk = [&](auto K) {
    constexpr int u0 = ord4(0, K.v), u1 = ord4(1, K.v);
    if constexpr (IS_DOWN) xf[K.v] = ldx(rs, lane * 16, ab + u0 * 1024 + khk * (u1 - u0));
    else xf[K.v] = ldf<K.v>(rs, lane * 16, ab);
  };
  SBAR();
  spin_ge<SY_READY, SLPC>(vs, ev + (IS_DOWN && !STAGED ? 1 : 0));
  STAMP(16 + 4 * ph);
  CDECL(); if (IS_DOWN) CST(4, 0);
  sfor<0, XP>([&](auto S) { ldk(S); });
  SBAR();
  v2u pl; unsigned pf;
  sfor<0, NOP>([&](auto N) {
    constexpr int n = N.v;
    const s16x8 cur = wf[n % PF];
    if constexpr (n + PF < NOP) {
      constexpr int a2 = (n + PF) >> 1;
      if constexpr (IS_DOWN && n + PF == 18) sigq(vq, pb + posk(9));
      if constexpr (((n + PF) & 15) == 10 && a2 + 3 < LAST) pl = peek_landed(vs);
      if constexpr (((n + PF) & 15) == 15 && a2 + 1 < LAST) take_landed(have, pl);
      if constexpr (((n + PF) & 15) == 0 && a2 < LAST) wait_landed(vs, have, pb + posk(a2 + 8 < LAST ? a2 + 8 : LAST));
      if constexpr (IS_DOWN && n + PF == 2 * T4 - 5) pf = peek(vs + 4 * (lane & 1), SY_T4);
      if constexpr (IS_DOWN && n + PF == 2 * T4 - 1) need_all<SY_T4>(pf, vs + 4 * (lane & 1), pass + 1);
      wf[n % PF] = rd(IC<n + PF>{});
    }
    acc[n & 1] = mfma32(cur, xf[n >> 1], acc[n & 1]);
    constexpr int NB = 2 * (24 - XP - E4) + 1;
    if constexpr (IS_DOWN && STAGED && n == NB - 6) pf = peek(vs, SY_READY);
    if constexpr (IS_DOWN && STAGED && n == NB - 1) need_all<SY_READY>(pf, vs, ev + 1);
    constexpr int k = n < 2 * E4 ? XP + n : ((n & 1) ? XP + E4 + (n >> 1) : NKS);
    if constexpr (k < NKS) ldk(IC<k>{});
    if constexpr (IS_DOWN) { if constexpr (n == 1) CST(4, 1); if constexpr (n == 15) CST(4, 2); if constexpr (n == 31) CST(4, 3); if constexpr (n == 47) CST(4, 4); if constexpr (n == 63) CST(4, 5); if constexpr (n == 75) CST(4, 6); if constexpr (n == 85) CST(4, 7); }
    SBAR();
  });
  asm volatile("" : "+v"(acc[0]), "+v"(acc[1]));
  sigq(vq, pb + (IS_DOWN ? P_END : posk(NKS)));
  const unsigned evo = ev + (IS_DOWN ? 2 : 1);
  if (!(IS_DOWN && pass == 1)) hold<IS_DOWN ? 4 : 2>(vs, evo);
  if (IS_DOWN) CDUMP(4);
  STAMP(17 + 4 * ph);
#pragma unroll
  for (int g = 0; g < 4; ++g) *(LAS f32x4*)(L + EX_OFF + wave * 4096 + g * 1024 + lane * 16) = f32x4{acc[1][4 * g], acc[1][4 * g + 1], acc[1][4 * g + 2], acc[1][4 * g + 3]};
  sig<SY_XF>(vsw, evo);
  spin_ge<SY_XF>(vs + 4 * (wave ^ 2), evo);
  unsigned o[8]; f32x2 xv[8];
#pragma unroll
  for (int g = 0; g < 4; ++g) {
    const f32x4 e = *(LAS f32x4*)(L + EX_OFF + (wave ^ 2) * 4096 + g * 1024 + lane * 16);
    xv[2 * g] = f32x2{acc[0][4 * g], acc[0][4 * g + 1]} + f32x2{e[0], e[1]};
    xv[2 * g + 1] = f32x2{acc[0][4 * g + 2], acc[0][4 * g + 3]} + f32x2{e[2], e[3]};
    o[2 * g] = pk(xv[2 * g]); o[2 * g + 1] = pk(xv[2 * g + 1]);
  }
  if (IS_DOWN && pass == 1) {
    GAS char* dst = (GAS char*)p.out + (size_t)(tb * 32 + (lane & 31)) * 1024 + (64 * jc + 32 * kh + 4 * h) * 2;
#pragma unroll
    for (int g = 0; g < 4; ++g) *(GAS v2u*)(dst + 16 * g) = v2u{o[2 * g], o[2 * g + 1]};
    STAMP(18 + 4 * ph);
    return;
  }
  const int xo = x_off + (tb * 32 + 4 * jc + 2 * kh) * 1024 + lane * 16;
  stx(v4u{o[0], o[1], o[2], o[3]}, rs, xo);
  stx(v4u{o[4], o[5], o[6], o[7]}, rs, xo + 1024);
  const f32x2 s2 = ((xv[0] + xv[1]) + (xv[2] + xv[3])) + ((xv[4] + xv[5]) + (xv[6] + xv[7]));
  const float sum = sum32(s2.x + s2.y);
  const f32x2 mi = sp(sum * (-1.f / 32.f));
  f32x2 q0 = {0.f, 0.f}, q1 = {0.f, 0.f};
#pragma unroll
  for (int i = 0; i < 4; ++i) { const f32x2 d0 = xv[2 * i] + mi, d1 = xv[2 * i + 1] + mi; q0 = fma2(d0, d0, q0); q1 = fma2(d1, d1, q1); }
  q0 += q1;
  const float m2s = sum32(q0.x + q0.y);
  if (lane < 32) stx2(v2u{asu(sum), asu(m2s)}, rs, lane * 8 + st_off + (tb * 16 + 2 * jc + kh) * 256);
  STAMP(18 + 4 * ph);
  publish(vsw, evo);
  STAMP(19 + 4 * ph);
}

DI void compute_main(const Params& p, LAS char* L, unsigned vs, int lane, int wave, int t, int j) {
  const __amdgpu_buffer_rsrc_t rs = __builtin_amdgcn_make_buffer_rsrc((void*)p.ws, 0, WS_BYTES, 0x00020000);
  unsigned have = 0;
  const unsigned vsw = vs + 4 * wave;
  STAMP(10);
  {
    const int c = lane >> 3, tk = lane & 7, odd = c & 1, blk = 4 * t + (j >> 2), sl = 8 * (j & 3) + tk;
    const GAS char* src = (const GAS char*)p.x + (size_t)(128 * t + 8 * j + tk) * 1024 + (16 * wave + c) * 16;
    v4u d[2];
    d[0] = *(const GAS v4u*)src; d[1] = *(const GAS v4u*)(src + 128);
    SBAR();
    STAMP(14);
#pragma unroll
    for (int a = 0; a < 2; ++a) {
      const int cI = 16 * wave + 8 * a + c;
      const unsigned r0 = ror8(odd ? d[a].x : d[a].z), r1 = ror8(odd ? d[a].y : d[a].w);
      const v4u o = odd ? v4u{r0, r1, d[a].z, d[a].w} : v4u{d[a].x, d[a].y, r0, r1};
      stx(o, rs, (cI >> 1) * 1024 + (sl + 32 * odd) * 16 + X_OFF + blk * 32768);
      float xv[8], s = 0.f;
#pragma unroll
      for (int i = 0; i < 4; ++i) { xv[2 * i] = bflo(d[a][i]); xv[2 * i + 1] = bfhi(d[a][i]); s += xv[2 * i] + xv[2 * i + 1]; }
      s += asf(ror8(asu(s))); s = sum16(s);
      const float mi = s * (1.f / 32.f); float m2 = 0.f;
#pragma unroll
      for (int i = 0; i < 8; ++i) { const float e = xv[i] - mi; m2 += e * e; }
      m2 += asf(ror8(asu(m2))); m2 = sum16(m2);
      if ((c & 3) == 0) stx2(v2u{asu(s), asu(m2)}, rs, (cI >> 2) * 256 + sl * 8 + ST_OFF + blk * 4096);
    }
    hold<0>(vs, 1);
    STAMP(11);
    publish(vsw, 1);
    STAMP(12);
  }
#pragma nounroll
  for (int pass = 0; pass < 2; ++pass) {
    const unsigned pb = pass * P_END, ph = 4 * pass + 1, eb = 5 * pass + 1;
    Resid res;
    const int xin = X_OFF + 2 * pass * X_SZ, stin = ST_OFF + 2 * pass * ST_SZ;
    const int tb = 4 * t + wave;
    {
      int ln = lane; asm volatile("" : "+v"(ln));
      const int h = ln >> 5, tok = ln & 31;
      CDECL();
      v4u xf[32], hx[16]; LnSteps lns;
      const int xb = opq(xin + tb * 32768), sb = opq(stin + tb * 4096);
      const int G = ln >> 4;
      const LB b1(L + ln * 16);
      f32x16 acc[3];
#pragma unroll
      for (int c = 0; c < 3; ++c)
#pragma unroll
        for (int i = 0; i < 16; ++i) acc[c][i] = 0.f;
      constexpr int PF = PFD;
      s16x8 wf[PF];
      const int oh = (16 * (wave >> 1) + (ln & 15) + 32 * (G & 1)) * 16 + (wave & 1) * 1024;
      const LB bh(L + oh + (G >> 1) * 3072);
      LAS char* bh2 = opqv(L + oh + (G >> 1) * 2048 + 3 * QA * 1024);
      auto rd = [&](auto N) -> s16x8 {
        constexpr int n = N.v;
        if constexpr (n >= 88 && n < 88 + QA / 2) return *(LAS s16x8*)bh.at<(n - 88) * 6144>();
        else if constexpr (n >= 88 && n < 104) return *(LAS s16x8*)(bh2 + (n - 88 - QA / 2) * 4096);
        else { constexpr int o = (n < 88 ? n : n - 16) * 1024; return *(LAS s16x8*)b1.at<o>(); }
      };
      f32x4 hacc = {0.f, 0.f, 0.f, 0.f};
      const unsigned vq = ln == 0 ? vsw + (SY_CONS - SY_OFF) : vs + (SY_DUMMY - SY_OFF) + 4 * ln, vs4 = vs + 4 * (ln & 3);
      LAS char* csb = opqv(L + CS1_OFF + 16 * h);
      LAS char* vw = opqv(L + EX_OFF + 2 * h * PLANE + (16 + 32 * wave + tok) * 16);
      LAS char* kw = opqv(L + KP_OFF + h * PLANE + (16 + 32 * wave + tok) * 16);
      LAS char* hxb = opqv(L + EX_OFF + ln * 16);
      wait_landed(vs, have, pb + 24);
      sfor<0, PF>([&](auto N) { wf[N.v] = rd(N); });
      SBAR();
      spin_ge<SY_READY, SLPC>(vs, eb);
      STAMP(16 + 4 * ph);
      CST(5, 0);
      v2u pl; unsigned pf, pc; f32x4 cs[12]; f32x4 csh; f32x2 lnh;
      sfor<0, XW>([&](auto S) { xf[S.v] = ldf<S.v>(rs, ln * 16, xb); });
      SBAR();
      CST(5, 1);
      auto op = [&](auto N) {
        constexpr int n = N.v, r = n + PF;
        const s16x8 cur = wf[n % PF];
        if constexpr (r == 104) sigq(vq, pb + KP_OFF / 1024);
        if constexpr (r < 112) wf[n % PF] = rd(IC<r>{});
        constexpr int st = n < 3 * QA ? n / 3 : QA + (n - 3 * QA) / 2, c = n < 3 * QA ? (n % 3 + 1) % 3 : 1 + (n - 3 * QA) % 2;
        if constexpr (n < 88) acc[c] = mfma32(cur, xf[st], acc[c]);
        else if constexpr (n < 104) hacc = mfma16(cur, hx[n - 88], hacc);
        else acc[0] = mfma32(cur, xf[n - 80], acc[0]);
      };
      sfor<0, 104>([&](auto N) {
        constexpr int n = N.v, r = n + PF;
        if constexpr (r % 24 == 17 && r < 72) pl = peek_landed(vs);
        if constexpr (r % 24 == 23 && r < 72) take_landed(have, pl);
        if constexpr (r % 24 == 0 && r < 96) wait_landed(vs, have, pb + r + 24);
        op(N);
        {
          constexpr int st_ = n / 3, k = n % 3 == 0 ? XW + st_ + (st_ < E1 ? st_ : E1) : (n % 3 == 1 && st_ < E1 ? XW + 2 * st_ + 1 : 32);
          if constexpr (k < 32) xf[k] = ldf<k>(rs, ln * 16, xb);
          if constexpr (n % 3 == 2 && n / 3 >= 4 && n / 3 < 12) lns.st[n / 3 - 4] = ldx2(rs, h * 2048 + tok * 8 + (n / 3 - 4) * 256, sb);
        }
        if constexpr (n == GATEN) sig<SY_RAW>(vsw, 1u);
        if constexpr (n >= 50 && n < 50 + LnSteps::N) lns.template step<n - 50>();
        if constexpr (n == 74) { pf = peek(vs, SY_LN); pc = peek(vs, SY_CS); }
        if constexpr (n == 79) need_all<SY_LN>(pf, vs, pass + 1);
        if constexpr (n == 81) need_all<SY_CS>(pc, vs, 1u);
        if constexpr (n >= 84 && n < 100) hx[n - 84] = *(LAS v4u*)(hxb + (n - 84) * 1024);
        if constexpr (n >= 100 && n < 104) cs[n - 100] = *(LAS f32x4*)(csb + (32 + 8 * (n - 100)) * 4);
        if constexpr (n == 2) CST(5, 2); if constexpr (n == 23) CST(5, 3); if constexpr (n == 47) CST(5, 4); if constexpr (n == 71) CST(5, 5); if constexpr (n == 87) CST(5, 6); if constexpr (n == 103) CST(5, 7);
        SBAR();
      });
      asm volatile("" : "+v"(acc[1]), "+v"(acc[2]), "+v"(hacc));
      STAMP(17 + 4 * ph);
      CST(1, 0);
      const f32x2 rstd = sp(lns.rstd), nmean = sp(lns.nmean);
      {
        unsigned pk_[8]; f32x2 vv[8];
        op(IC<104>{});
#pragma unroll
        for (int i = 4; i < 8; ++i) cs[i] = *(LAS f32x4*)(csb + (32 + 8 * i) * 4);
        csh = *(LAS f32x4*)(L + CS1_OFF + (32 * (1 + (wave & 1)) + 16 * (wave >> 1) + 4 * G) * 4); lnh = *(LAS f32x2*)(L + LN_OFF + (ln & 15) * 8);
        SBAR(); op(IC<105>{}); SBAR();
        sfor<0, 8>([&](auto I) { constexpr int i = I.v; pk_[i] = pk(fma2(f32x2{cs[i >> 1][2 * (i & 1)], cs[i >> 1][2 * (i & 1) + 1]}, nmean, pr<i>(acc[1])) * rstd); });
        pf = peek(vs4, SY_CONS);
        SBAR(); op(IC<106>{}); op(IC<107>{}); SBAR();
        sfor<0, 8>([&](auto I) { constexpr int i = I.v; vv[i] = fma2(f32x2{cs[4 + (i >> 1)][2 * (i & 1)], cs[4 + (i >> 1)][2 * (i & 1) + 1]}, nmean, pr<i>(acc[2])) * rstd; });
        const f32x2 hr = sp(t == 0 ? 0.f : lnh.x), hm = sp(t == 0 ? 0.f : lnh.y);
        f32x2 hv0 = fma2(f32x2{csh[0], csh[1]}, hm, f32x2{hacc[0], hacc[1]}) * hr, hv1 = fma2(f32x2{csh[2], csh[3]}, hm, f32x2{hacc[2], hacc[3]}) * hr;
        if (t == 0) { hv0 = sp(0.f); hv1 = sp(0.f); }
        SBAR();
        CST(1, 1);
        need_all<SY_CONS>(pf, vs4, pb + KP_OFF / 1024);
        op(IC<108>{});
        *(LAS v4u*)kw = v4u{pk_[0], pk_[1], pk_[2], pk_[3]};
        *(LAS v4u*)(kw + 2 * PLANE) = v4u{pk_[4], pk_[5], pk_[6], pk_[7]};
        SBAR(); op(IC<109>{}); SBAR();
        if (wave & 1) *(LAS f32x4*)(L + EX_OFF + ((wave >> 1) * 4 + 2 * (G & 1) + (G >> 1)) * PLANE + (ln & 15) * 16) = f32x4{hv0.x, hv0.y, hv1.x, hv1.y};
        else *(LAS v2u*)(L + KP_OFF + ((wave >> 1) * 2 + (G & 1)) * PLANE + (ln & 15) * 16 + 8 * (G >> 1)) = v2u{pk(hv0), pk(hv1)};
        sig<SY_KV>(vsw, 2 * pass + 1);
        SBAR(); op(IC<110>{}); SBAR();
#pragma unroll
        for (int g = 0; g < 4; ++g) *(LAS f32x4*)(vw + ((g >> 1) * 4 + (g & 1)) * PLANE) = f32x4{vv[2 * g].x, vv[2 * g].y, vv[2 * g + 1].x, vv[2 * g + 1].y};
        sig<SY_KV>(vsw, 2 * pass + 2);
        SBAR(); op(IC<111>{});
#pragma unroll
        for (int i = 8; i < 12; ++i) cs[i] = *(LAS f32x4*)(csb + 8 * (i - 8) * 4);
        res = ld_resid(rs, ln, wave, t, j, xin);
        SBAR();
      }
      CST(1, 2);
      spin_all_ge<SY_KV>(vs4, 2 * pass + 1);
      CST(1, 3);
      LAS char* kb = L + KP_OFF + h * 2 * PLANE + (16 + 32 * wave + tok - 14) * 16;
      LAS char* vb = L + EX_OFF + h * 4 * PLANE + (16 + 32 * wave + tok - 14) * 16;
      float lg[8]; v4u kk[16]; f32x4 vr[32];
#pragma unroll
      for (int w = 0; w < 8; ++w) { kk[2 * w] = *(LAS v4u*)(kb + (14 - 2 * w) * 16); kk[2 * w + 1] = *(LAS v4u*)(kb + PLANE + (14 - 2 * w) * 16); }
      SBAR();
      unsigned qq[8];
      {
        unsigned pq[8];
        sfor<0, 8>([&](auto I) { constexpr int i = I.v; pq[i] = pk(fma2(f32x2{cs[8 + (i >> 1)][2 * (i & 1)], cs[8 + (i >> 1)][2 * (i & 1) + 1]}, nmean, pr<i>(acc[0]))); });
#pragma unroll
        for (int i = 0; i < 4; ++i) { auto r = __builtin_amdgcn_permlane32_swap(pq[i], pq[4 + i], false, false); qq[i] = r[0]; qq[4 + i] = r[1]; }
      }
      pf = peek(vs4, SY_KV);
      SBAR();
      CST(1, 4);
      need_all<SY_KV>(pf, vs4, 2 * pass + 2);
      sfor<0, 4>([&](auto W) {
        constexpr int w = 2 * W.v;
#pragma unroll
        for (int g = 0; g < 8; ++g) vr[4 * w + g] = *(LAS f32x4*)(vb + (g & 3) * PLANE + (14 - 2 * (w + (g >> 2))) * 16);
        float a = dot2z(qq[0], kk[2 * w].x), b = dot2z(qq[0], kk[2 * w + 2].x);
#pragma unroll
        for (int i = 1; i < 8; ++i) { a = dot2(qq[i], kk[2 * w + (i >> 2)][i & 3], a); b = dot2(qq[i], kk[2 * w + 2 + (i >> 2)][i & 3], b); }
        lg[w] = a; lg[w + 1] = b;
        SBAR();
      });
      asm volatile("" : "+v"(lg[0]), "+v"(lg[1]), "+v"(lg[2]), "+v"(lg[3]), "+v"(lg[4]), "+v"(lg[5]), "+v"(lg[6]), "+v"(lg[7]));
      sigq(vq, pb + P_W2);
      hold<1>(vs, eb + 1);
      CST(1, 6);
      if (t == 0 && wave == 0) {
#pragma unroll
        for (int w = 1; w < 8; ++w) lg[w] = tok >= 2 * w ? lg[w] : -1e30f;
      }
      const float mx = max3(max3(lg[0], lg[1], lg[2]), max3(lg[3], lg[4], lg[5]), max3(lg[6], lg[7], lg[7]));
      const float sc = lns.rstd * (0.25f * 1.4426950408889634f);
      f32x2 e2[4];
#pragma unroll
      for (int w = 0; w < 4; ++w) { const f32x2 z = (f32x2{lg[2 * w], lg[2 * w + 1]} - sp(mx)) * sp(sc); e2[w] = f32x2{__builtin_amdgcn_exp2f(z.x), __builtin_amdgcn_exp2f(z.y)}; }
      const f32x2 s2 = (e2[0] + e2[1]) + (e2[2] + e2[3]);
      const f32x2 inv = sp(__builtin_amdgcn_rcpf(s2.x + s2.y));
      f32x2 ov[8];
#pragma unroll
      for (int w = 0; w < 8; ++w) {
        const f32x2 pw = sp(e2[w >> 1][w & 1]);
#pragma unroll
        for (int g = 0; g < 4; ++g) {
          const f32x4 v = vr[4 * w + g];
          ov[2 * g] = w ? fma2(f32x2{v[0], v[1]}, pw, ov[2 * g]) : f32x2{v[0], v[1]} * pw;
          ov[2 * g + 1] = w ? fma2(f32x2{v[2], v[3]}, pw, ov[2 * g + 1]) : f32x2{v[2], v[3]} * pw;
        }
      }
      const int oo = O_OFF + pass * X_SZ + (tb * 32 + 2 * j) * 1024 + h * 1024 + tok * 16;
      stx(v4u{pk(ov[0] * inv), pk(ov[1] * inv), pk(ov[2] * inv), pk(ov[3] * inv)}, rs, oo);
      stx(v4u{pk(ov[4] * inv), pk(ov[5] * inv), pk(ov[6] * inv), pk(ov[7] * inv)}, rs, oo + 512);
      CST(1, 7); CDUMP(1); CDUMP(5);
      STAMP(18 + 4 * ph);
      publish(vsw, eb + 1);
      STAMP(19 + 4 * ph);
    }
    { int ln = lane; asm volatile("" : "+v"(ln));
      proj_phase<16, false>(p, L, vs, rs, ln, wave, t, j, pass, have, O_OFF + pass * X_SZ, res, xin + X_SZ, stin + ST_SZ, eb + 1, ph + 1); }
    {
      int ln = lane; asm volatile("" : "+v"(ln));
      const int h = ln >> 5, tok = ln & 31;
      CDECL();
      v4u xf[32]; LnSteps lns;
      const int xb = opq(xin + X_SZ + tb * 32768), sb = opq(stin + ST_SZ + tb * 4096);
      const LB b3(L + ln * 16);
      f32x16 acc[6];
#pragma unroll
      for (int c = 0; c < 6; ++c)
#pragma unroll
        for (int i = 0; i < 16; ++i) acc[c][i] = 0.f;
      constexpr int PF = PFD;
      s16x8 wf[PF];
      auto rd = [&](auto N) -> s16x8 { return *(LAS s16x8*)b3.template at<slot_of(P_W3 + N.v)>(); };
      const unsigned vq = ln == 0 ? vsw + (SY_CONS - SY_OFF) : vs + (SY_DUMMY - SY_OFF) + 4 * ln;
      wait_landed(vs, have, pb + pos3(4));
      sfor<0, PF>([&](auto N) { wf[N.v] = rd(N); });
      SBAR();
      spin_ge<SY_READY, SLPC>(vs, eb + 2);
      v2u pl; unsigned pc;
      STAMP(16 + 4 * (ph + 2));
      CST(3, 0);
      sfor<0, XW>([&](auto S) { xf[S.v] = ldf<S.v>(rs, ln * 16, xb); });
      SBAR();
      sfor<0, 192>([&](auto N) {
        constexpr int n = N.v;
        const s16x8 cur = wf[n % PF];
        if constexpr (n + PF < 192) {
          if constexpr (((n + PF) % 12 == 2 && n + PF > 12) || (n + PF) == 188) sigq(vq, pb + pos3((n + PF) / 6));
          if constexpr (n == 100) pc = peek(vs, SY_CS);
          if constexpr (n == 106) need_all<SY_CS>(pc, vs, 2u);
          if constexpr (!(AB & 8) && (n + PF) % 24 == 14 && n + PF < 24 * 7) pl = peek_landed(vs);
          if constexpr (!(AB & 8) && (n + PF) % 24 == 20 && n + PF < 24 * 7) take_landed(have, pl);
          if constexpr (!(AB & 8) && (n + PF) % 24 == 22 && n + PF < 24 * 7) wait_landed(vs, have, pb + pos3((n + PF) / 6 + 5));
          wf[n % PF] = rd(IC<n + PF>{});
        }
        acc[n % 6] = mfma32(cur, xf[n / 6], acc[n % 6]);
        {
          constexpr int st_ = n / 6, k = n % 6 == 0 ? XW + st_ + (st_ < E3 ? st_ : E3) : (n % 6 == 4 && st_ < E3 ? XW + 2 * st_ + 1 : 32);
          if constexpr (k < 32) { if constexpr (AB & 1) xf[k] = xf[k % 8]; else xf[k] = ldf<k>(rs, ln * 16, xb); }
        }
        if constexpr (!(AB & 2) && n % 6 == 2 && n / 6 < 8) lns.st[n / 6] = ldx2(rs, h * 2048 + tok * 8 + (n / 6) * 256, sb);
        if constexpr (!(AB & 2) && n >= 90 && n % 3 == 0 && (n - 90) / 3 < LnSteps::N) lns.template step<(n - 90) / 3>();
        if constexpr (n == 5) CST(3, 1); if constexpr (n == 47) CST(3, 2); if constexpr (n == 95) CST(3, 3); if constexpr (n == 119) CST(3, 4); if constexpr (n == 143) CST(3, 5); if constexpr (n == 167) CST(3, 6); if constexpr (n == 191) CST(3, 7);
        SBAR();
      });
      asm volatile("" : "+v"(acc[0]), "+v"(acc[1]), "+v"(acc[2]), "+v"(acc[3]), "+v"(acc[4]), "+v"(acc[5]));
      sig<SY_CONS>(vsw, pb + P_W4);
      CDUMP(3);
      STAMP(17 + 4 * (ph + 2));
      const f32x2 k1 = sp(lns.rstd * -2.3022081985f), k3 = sp(lns.rstd * lns.rstd * lns.rstd * -0.1029432397f), sd2 = sp(lns.sd * lns.sd), nmean = sp(lns.nmean);
      const int us = (j / 3) * 16 + (j % 3) * 6, nu = (j % 3 == 2) ? 4 : 6;
      const int ho = H_OFF + pass * H_SZ + (tb * 86 + us) * 1024 + ln * 16;
#pragma unroll
      for (int u = 0; u < 6; ++u) {
        if (u < 4 || nu == 6) {
          unsigned hv[4];
#pragma unroll
          for (int g = 0; g < 2; ++g) {
            const f32x4 cg = *(LAS f32x4*)(L + CS3_OFF + (32 * u + 8 * g + 4 * h) * 4), cu = *(LAS f32x4*)(L + CS3_OFF + (32 * u + 16 + 8 * g + 4 * h) * 4);
#pragma unroll
            for (int i = 0; i < 2; ++i) {
              const f32x2 tg = fma2(f32x2{cg[2 * i], cg[2 * i + 1]}, nmean, f32x2{acc[u][4 * g + 2 * i], acc[u][4 * g + 2 * i + 1]});
              const f32x2 tu = fma2(f32x2{cu[2 * i], cu[2 * i + 1]}, nmean, f32x2{acc[u][8 + 4 * g + 2 * i], acc[u][8 + 4 * g + 2 * i + 1]});
              const f32x2 z = tg * fma2(tg * tg, k3, k1);
              const f32x2 d = fma2(f32x2{__builtin_amdgcn_exp2f(z.x), __builtin_amdgcn_exp2f(z.y)}, sd2, sd2);
              hv[2 * g + i] = pk(tg * tu * f32x2{__builtin_amdgcn_rcpf(d.x), __builtin_amdgcn_rcpf(d.y)});
            }
          }
          stx(v4u{hv[0], hv[1], hv[2], hv[3]}, rs, ho + u * 1024);
          if (u == 3) { asm volatile("s_waitcnt vmcnt(1)" ::: "memory"); sig<SY_PROG>(vsw, eb + 3); }
        }
        SBAR();
      }
      res = ld_resid(rs, ln, wave, t, j, xin + X_SZ);
      STAMP(18 + 4 * (ph + 2));
      hold<3>(vs, eb + 4);
      publish(vsw, eb + 4);
      STAMP(19 + 4 * (ph + 2));
    }
    { int ln = lane; asm volatile("" : "+v"(ln));
      proj_phase<43, true>(p, L, vs, rs, ln, wave, t, j, pass, have, H_OFF + pass * H_SZ, res, xin + 2 * X_SZ, stin + 2 * ST_SZ, eb + 3, ph + 3); }
  }
}

__global__ void __launch_bounds__(512, 2) fused_kernel(Params p) {
  extern __shared__ __attribute__((aligned(16))) char smem[];
  LAS char* L = (LAS char*)smem;
  const int lane = threadIdx.x & 63, wave = __builtin_amdgcn_readfirstlane(threadIdx.x >> 6);
  const int b = blockIdx.x;
  const int t = ((b & 7) >> 1) * 4 + (b >> 6), j = (b & 1) * 8 + ((b >> 3) & 7);
  asm volatile(".subsection 2\n\t.fill 128, 4, 0xBF800000\n\t.subsection 0");
  if (threadIdx.x < 44) *(LAS unsigned*)(L + SY_OFF + threadIdx.x * 4) = 0u;
  __syncthreads();
  if (wave == 7) {
    u64 pc; asm volatile("s_getpc_b64 %0" : "=s"(pc));
    const GAS char* c = (const GAS char*)(pc & ~255ull) + (b >> 3) * CODEPF + lane * 128;
    unsigned x = 0;
    if (lane * 128 < CODEPF) x = *(const GAS unsigned*)c;
    asm volatile("" :: "v"(x));
  }
  const unsigned lb = (unsigned)(size_t)L;
  unsigned vs = lb + SY_OFF; asm volatile("" : "+v"(vs));
  if (wave < 4) { __builtin_amdgcn_s_setprio(2); compute_main(p, L, vs, lane, wave, t, j); }
  else if (wave == 4) { __builtin_amdgcn_s_setprio(3); poller_main(p, L, vs, lane, t, j, wave); }
  else if (wave == 5) { __builtin_amdgcn_s_setprio(3); helper_main(p, L, lb, vs, lane, t, j); }
  else {
    loader_main(p, L, lb, lane, t, j, wave - 6, wave); }
  PROF_DUMP();
}
}

extern "C" void kernel_launch(void* const* d_in, const int* in_sizes, int n_in, void* d_out, int out_size, void* d_ws, size_t ws_size, hipStream_t stream) {
  Params p;
  p.x = (const u16*)d_in[0]; p.wqkv = (const u16*)d_in[1]; p.wo = (const u16*)d_in[2];
  p.wg = (const u16*)d_in[4]; p.wu = (const u16*)d_in[5]; p.wd = (const u16*)d_in[6];
  p.out = (u16*)d_out; p.ws = (unsigned char*)d_ws;
  (void)hipFuncSetAttribute((const void*)fused_kernel, hipFuncAttributeMaxDynamicSharedMemorySize, LDS_ALLOC);
  hipLaunchKernelGGL(fused_kernel, dim3(256), dim3(512), LDS_ALLOC, stream, p);
}
```

```cpp
#include <hip/hip_runtime.h>
#include <hip/hip_bf16.h>

#define GAS __attribute__((address_space(1)))
#define LAS __attribute__((address_space(3)))
#define DI __device__ __forceinline__
typedef unsigned v4u __attribute__((ext_vector_type(4)));
typedef unsigned v2u __attribute__((ext_vector_type(2)));
typedef float f32x2 __attribute__((ext_vector_type(2)));
typedef float f32x4 __attribute__((ext_vector_type(4)));
typedef float f32x16 __attribute__((ext_vector_type(16)));
typedef short s16x4 __attribute__((ext_vector_type(4)));
typedef short s16x8 __attribute__((ext_vector_type(8)));
typedef __bf16 bf16x8 __attribute__((ext_vector_type(8)));
typedef __bf16 bf16x2 __attribute__((ext_vector_type(2)));
typedef unsigned long long u64;
typedef unsigned short u16;
#define RLX_AGENT __ATOMIC_RELAXED, __HIP_MEMORY_SCOPE_AGENT

namespace {
constexpr int AUX_SC1 = 16;
constexpr int FLAG_OFF = 0;
constexpr int EPOCH_OFF = 65536;
constexpr int CSF_OFF = 131072;
constexpr int CSP_OFF = 262144;
constexpr int X_OFF = 1 << 20, X_SZ = 2 << 20;
constexpr int O_OFF = X_OFF + 4 * X_SZ;
constexpr int ST_OFF = O_OFF + 2 * X_SZ, ST_SZ = 64 * 16 * 256;
constexpr int H_OFF = ST_OFF + 4 * ST_SZ, H_SZ = 64 * 86 * 1024;
constexpr int WT1_OFF = H_OFF + 2 * H_SZ, WT3_OFF = WT1_OFF + 16 * 96 * 1024, WT2_OFF = WT3_OFF + 16 * 192 * 1024, WT4_OFF = WT2_OFF + 8 * 64 * 1024;
constexpr int WS_BYTES = WT4_OFF + 8 * 172 * 1024;
constexpr int PROF_OFF = 32 << 20;
static_assert(WS_BYTES <= PROF_OFF, "");
constexpr int NR = 136;
constexpr int EX_OFF = NR * 1024, PLANE = 144 * 16;
constexpr int CS1_OFF = EX_OFF + 8 * PLANE, CS3_OFF = CS1_OFF + 96 * 4;
constexpr int LN_OFF = CS3_OFF + 192 * 4;
constexpr int KP_OFF = 79 * 1024;
constexpr int SY_OFF = LN_OFF + 16 * 8;
constexpr int SY_LANDED = SY_OFF;
constexpr int SY_CONS = SY_OFF + 16;
constexpr int SY_WT = SY_OFF + 32, SY_RAW = SY_OFF + 144;
constexpr int SY_HOLD = SY_OFF + 40, SY_READY = SY_OFF + 44;
constexpr int SY_PROG = SY_OFF + 48;
constexpr int SY_KV = SY_OFF + 64;
constexpr int SY_XF = SY_OFF + 80;
constexpr int SY_CS = SY_OFF + 96;
constexpr int SY_T4 = SY_OFF + 104;
constexpr int SY_LN = SY_OFF + 112, SY_T2 = SY_OFF + 116, SY_SR = SY_OFF + 160;
constexpr int SY_TC = SY_OFF + 128;
constexpr int SY_DUMMY = SY_OFF + 176;
constexpr int LDS_BYTES = SY_DUMMY + 256;
constexpr int P_W1 = 0, P_W2 = 96, P_W3 = 160, P_W4 = 352, P_END = 544;
constexpr int QA = 24;
constexpr int pos1(int s, int c  ) { return s < QA ? 3 * s + c : (c < 2 ? 3 * QA + 2 * (s - QA) + c : 64 + s); }
constexpr int pos2(int a) { return P_W2 + 4 * a; }
constexpr int pos3(int s) { return P_W3 + 6 * s; }
constexpr int pos4(int a) { return P_W4 + 4 * a; }
constexpr int T4 = 34;
constexpr int ustart(int j) { return (j / 3) * 16 + (j % 3) * 6; }
constexpr bool early_unit(int kh, int u) { for (int i = 0; i < 8; ++i) if (u >= ustart(8 * kh + i) && u < ustart(8 * kh + i) + 3) return true; return false; }
constexpr int ord4(int kh, int a) {
  if (a < 24) return ustart(8 * kh + a / 3) + a % 3;
  int n = 24;
  for (int u = 43 * kh; u < 43 * kh + 43; ++u) if (!early_unit(kh, u)) { if (n == a) return u; ++n; }
  return -1;
}
static_assert(ord4(0, 42) == 42 && ord4(1, 24) == 43 && ord4(1, 42) == 85, "unit order");
constexpr int slot_of(int p) { return (p % NR) * 1024; }

struct Params { const u16* x; const u16* wqkv; const u16* wo; const u16* wg; const u16* wu; const u16* wd; u16* out; unsigned char* ws; };

DI unsigned rfl(unsigned v) { return __builtin_amdgcn_readfirstlane(v); }
DI u64 uni64(u64 e) { return ((u64)rfl((unsigned)(e >> 32)) << 32) | rfl((unsigned)e); }
DI float asf(unsigned u) { return __builtin_bit_cast(float, u); }
DI unsigned asu(float f) { return __builtin_bit_cast(unsigned, f); }
DI unsigned pk(float a, float b) { bf16x2 v = {(__bf16)a, (__bf16)b}; return __builtin_bit_cast(unsigned, v); }
DI float bflo(unsigned u) { return asf(u << 16); }
DI float bfhi(unsigned u) { return asf(u & 0xffff0000u); }
DI float rbf(float a) { return bflo(pk(a, a)); }
DI float dot2(unsigned a, unsigned b, float c) { return __builtin_amdgcn_fdot2_f32_bf16(__builtin_bit_cast(bf16x2, a), __builtin_bit_cast(bf16x2, b), c, false); }
DI float max3(float a, float b, float c) { return __builtin_fmaxf(__builtin_fmaxf(a, b), c); }
DI float add1(float a, float b) { float r; asm("v_add_f32 %0, %1, %2" : "=v"(r) : "v"(a), "v"(b)); return r; }
DI float add3(float a, float b, float c) { return __builtin_constant_p(c) ? add1(a, b) : add1(add1(a, b), c); }
DI f32x2 fma2(f32x2 a, f32x2 b, f32x2 c) { return __builtin_elementwise_fma(a, b, c); }
DI f32x2 sp(float a) { return f32x2{a, a}; }
DI unsigned pk(f32x2 v) { return pk(v.x, v.y); }
template <int I> DI f32x2 pr(const f32x16& v) { return f32x2{v[2 * I], v[2 * I + 1]}; }
DI float sum32(float x) { auto r = __builtin_amdgcn_permlane32_swap(asu(x), asu(x), false, false); return asf(r[0]) + asf(r[1]); }
DI float sum16(float x) { auto r = __builtin_amdgcn_permlane16_swap(asu(x), asu(x), false, false); return asf(r[0]) + asf(r[1]); }
DI unsigned ror8(unsigned x) { return (unsigned)__builtin_amdgcn_update_dpp(0, (int)x, 0x128, 0xf, 0xf, false); }
DI f32x16 mfma32(s16x8 a, v4u b, f32x16 c) { return __builtin_amdgcn_mfma_f32_32x32x16_bf16(__builtin_bit_cast(bf16x8, a), __builtin_bit_cast(bf16x8, b), c, 0, 0, 0); }
DI f32x4 mfma16(s16x8 a, v4u b, f32x4 c) { return __builtin_amdgcn_mfma_f32_16x16x32_bf16(__builtin_bit_cast(bf16x8, a), __builtin_bit_cast(bf16x8, b), c, 0, 0, 0); }
DI s16x4 trr(LAS char* p) { return __builtin_amdgcn_ds_read_tr16_b64_v4i16((LAS s16x4*)p); }
DI s16x8 cat(s16x4 a, s16x4 b) { return __builtin_shufflevector(a, b, 0, 1, 2, 3, 4, 5, 6, 7); }
#define STR2(x) #x
#define STR(x) STR2(x)
#define PRE 16
#define MASKP 1
#define GAP 3
#define PRE1 24
#define PREA 16
#define PREB 16
#define PREW 24
#define GAPW 6
#define TOUCHW 7
#define SLPS 8
#define SLPP 1
#define SLPC 1
#define SLPL 3
#define CBAR() asm volatile("" ::: "memory")
#define SBAR() __builtin_amdgcn_sched_barrier(0)
#define DRAIN() asm volatile("s_waitcnt vmcnt(0)" ::: "memory")

#define STAMP(i) do {} while (0)
#define PROF_DUMP() do {} while (0)
constexpr int LDS_ALLOC = LDS_BYTES;

#define CDECL() do {} while (0)
#define CST(site, i) do {} while (0)
#define CDUMP(site) do {} while (0)
template <int I> struct IC { static constexpr int v = I; };
template <int B, int E, class F> DI void sfor(F&& f) { if constexpr (B < E) { f(IC<B>{}); sfor<B + 1, E>(f); } }
DI int opq(int x) { asm volatile("" : "+s"(x)); return x; }
DI LAS char* opqv(LAS char* x) { asm volatile("" : "+v"(x)); return x; }

template <int OFF, int SLP = 0> DI void spin_ge(unsigned vs, unsigned need) {
  unsigned tv, ts, cnt;
  asm volatile("s_mov_b32 %2, 0x40000\n.Lsp%=:\n\tds_read_b32 %0, %3 offset:%5\n\ts_waitcnt lgkmcnt(0)\n\tv_readfirstlane_b32 %1, %0\n\t"
               "s_cmp_ge_u32 %1, %4\n\ts_cbranch_scc1 .Lsd%=\n\t.if %6\n\ts_sleep %6\n\t.endif\n\ts_sub_u32 %2, %2, 1\n\ts_cbranch_scc0 .Lsp%=\n.Lsd%=:"
               : "=&v"(tv), "=&s"(ts), "=&s"(cnt) : "v"(vs), "s"(need), "i"(OFF - SY_OFF), "i"(SLP) : "memory", "scc");
}
template <int OFF, int SLP = 0> DI void spin_all_ge(unsigned vs4, unsigned need) {
  unsigned tv, cnt;
  asm volatile("s_mov_b32 %1, 0x40000\n.Lsp%=:\n\tds_read_b32 %0, %2 offset:%4\n\ts_waitcnt lgkmcnt(0)\n\tv_cmp_gt_u32 vcc, %3, %0\n\t"
               "s_cbranch_vccz .Lsd%=\n\t.if %5\n\ts_sleep %5\n\t.endif\n\ts_sub_u32 %1, %1, 1\n\ts_cbranch_scc0 .Lsp%=\n.Lsd%=:"
               : "=&v"(tv), "=&s"(cnt) : "v"(vs4), "s"(need), "i"(OFF - SY_OFF), "i"(SLP) : "memory", "scc", "vcc");
}
DI void wait_landed(unsigned vs, unsigned& have, unsigned need) {
  unsigned t0, t1, cnt;
  asm volatile("s_cmp_ge_u32 %0, %5\n\ts_cbranch_scc0 .Lslow%=\n.Lback%=:\n\t.subsection 1\n.Lslow%=:\n\ts_mov_b32 %3, 0x40000\n.Lsp%=:\n\t"
               "ds_read_b32 %1, %4 offset:%6\n\tds_read_b32 %2, %4 offset:%6+4\n\ts_waitcnt lgkmcnt(0)\n\tv_min_u32 %1, %1, %2\n\ts_nop 0\n\tv_readfirstlane_b32 %0, %1\n\t"
               "s_cmp_ge_u32 %0, %5\n\ts_cbranch_scc1 .Lback%=\n\ts_sub_u32 %3, %3, 1\n\ts_cbranch_scc0 .Lsp%=\n\ts_branch .Lback%=\n\t.subsection 0"
               : "+s"(have), "=&v"(t0), "=&v"(t1), "=&s"(cnt) : "v"(vs), "s"(need), "i"(SY_LANDED - SY_OFF) : "memory", "scc");
}
template <int OFF> DI void sig(unsigned vs, unsigned val) {
  asm volatile("s_mov_b64 exec, 1\n\tds_write_b32 %0, %1 offset:%2\n\ts_mov_b64 exec, -1" :: "v"(vs), "v"(val), "i"(OFF - SY_OFF) : "memory");
}
DI void sigq(unsigned vq, unsigned val) { asm volatile("ds_write_b32 %0, %1" :: "v"(vq), "v"(val) : "memory"); }

DI unsigned peek(unsigned addr, int off) { return *(volatile LAS unsigned*)(size_t)(addr + off - SY_OFF); }
template <int OFF> DI void need_all(unsigned seen, unsigned vs4, unsigned need) {
  unsigned tv, cnt;
  asm volatile("v_cmp_gt_u32 vcc, %3, %5\n\ts_cbranch_vccnz .Lslow%=\n.Lback%=:\n\t.subsection 1\n.Lslow%=:\n\ts_mov_b32 %1, 0x40000\n.Lsp%=:\n\t"
               "ds_read_b32 %0, %2 offset:%4\n\ts_waitcnt lgkmcnt(0)\n\tv_cmp_gt_u32 vcc, %3, %0\n\t"
               "s_cbranch_vccz .Lback%=\n\ts_sub_u32 %1, %1, 1\n\ts_cbranch_scc0 .Lsp%=\n\ts_branch .Lback%=\n\t.subsection 0"
               : "=&v"(tv), "=&s"(cnt) : "v"(vs4), "s"(need), "i"(OFF - SY_OFF), "v"(seen) : "memory", "scc", "vcc");
}
DI v2u peek_landed(unsigned vs) { return *(volatile LAS v2u*)(size_t)(vs + SY_LANDED - SY_OFF); }
DI void take_landed(unsigned& have, v2u v) { asm volatile("s_max_u32 %0, %0, %1" : "+s"(have) : "s"(rfl(min(v.x, v.y))) : "scc"); }
DI void publish(unsigned vsw, unsigned ev) { DRAIN(); sig<SY_PROG>(vsw, ev); }

DI v4u ln_stats(const v4u (&st)[4]) {
  f32x2 s = {0.f, 0.f};
#pragma unroll
  for (int i = 0; i < 4; ++i) s += f32x2{asf(st[i].x), asf(st[i].z)};
  const f32x2 mean = f32x2{sum32(sum16(s.x)), sum32(sum16(s.y))} * sp(1.f / 512.f);
  f32x2 m2 = {0.f, 0.f};
#pragma unroll
  for (int i = 0; i < 4; ++i) { const f32x2 d = fma2(f32x2{asf(st[i].x), asf(st[i].z)}, sp(1.f / 32.f), -mean); m2 += fma2(d * sp(32.f), d, f32x2{asf(st[i].y), asf(st[i].w)}); }
  const float v0 = sum32(sum16(m2.x)) * (1.f / 512.f), v1 = sum32(sum16(m2.y)) * (1.f / 512.f);
  const float sd0 = rbf(__builtin_amdgcn_sqrtf(rbf(rbf(v0) + 1e-5f))), sd1 = rbf(__builtin_amdgcn_sqrtf(rbf(rbf(v1) + 1e-5f)));
  return v4u{asu(__builtin_amdgcn_rcpf(sd0)), asu(-rbf(mean.x)), asu(__builtin_amdgcn_rcpf(sd1)), asu(-rbf(mean.y))};
}
struct LnSteps {
  v2u st[8]; f32x2 a, b, c, d; float S, R, m16, rstd, nmean, sd;
  static constexpr int N = 22;
  DI f32x2 f(int i) const { return f32x2{asf(st[i].x), asf(st[i].y)}; }
  template <int I> DI void step() {
    if constexpr (I == 0) { a.x = add3(asf(st[0].x), asf(st[1].x), asf(st[2].x)); b.x = add3(asf(st[3].x), asf(st[4].x), asf(st[5].x)); }
    else if constexpr (I == 1) { a.y = add3(asf(st[0].y), asf(st[1].y), asf(st[2].y)); b.y = add3(asf(st[3].y), asf(st[4].y), asf(st[5].y)); }
    else if constexpr (I == 2) { a.x = add3(add3(a.x, asf(st[6].x), asf(st[7].x)), b.x, 0.f); }
    else if constexpr (I == 3) { a.y = add3(add3(a.y, asf(st[6].y), asf(st[7].y)), b.y, 0.f); }
    else if constexpr (I == 4) { S = sum32(a.x); }
    else if constexpr (I == 5) { m16 = S * (1.f / 16.f); nmean = S * (-1.f / 512.f); }
    else if constexpr (I == 6) { const float e = asf(st[0].x) - m16; R = e * e; }
    else if constexpr (I < 14) { const float e = asf(st[I - 6].x) - m16; R = __builtin_fmaf(e, e, R); }
    else if constexpr (I == 14) { R = __builtin_fmaf(R, 1.f / 32.f, a.y); }
    else if constexpr (I == 15) { R = sum32(R); }
    else if constexpr (I == 16) { R = rbf(R * (1.f / 512.f)); }
    else if constexpr (I == 17) { R = rbf(R + 1e-5f); }
    else if constexpr (I == 18) { R = __builtin_amdgcn_sqrtf(R); }
    else if constexpr (I == 19) { R = rbf(R); }
    else if constexpr (I == 20) { sd = R; rstd = __builtin_amdgcn_rcpf(R); }
    else if constexpr (I == 21) { nmean = rbf(nmean); }

    if constexpr (I == 4) asm volatile("" : "+v"(S)); if constexpr (I == 5) asm volatile("" : "+v"(m16), "+v"(nmean));
    if constexpr (I >= 6 && I < 20) asm volatile("" : "+v"(R)); if constexpr (I == 20) asm volatile("" : "+v"(rstd)); if constexpr (I == 21) asm volatile("" : "+v"(nmean));
  }
};
DI v4u ldx(__amdgpu_buffer_rsrc_t rs, int voff, int soff) { return __builtin_amdgcn_raw_buffer_load_b128(rs, voff, soff, AUX_SC1); }
DI v2u ldx2(__amdgpu_buffer_rsrc_t rs, int voff, int soff) { return __builtin_amdgcn_raw_buffer_load_b64(rs, voff, soff, AUX_SC1); }

#define MAPB 1
#define AB 0
#define DEPTH1 20
#define NE1 0
#define W1GATE SY_SWEPT
DI void stx(v4u v, __amdgpu_buffer_rsrc_t rs, int voff) { __builtin_amdgcn_raw_buffer_store_b128(v, rs, voff, 0, AUX_SC1); }
DI void stx2(v2u v, __amdgpu_buffer_rsrc_t rs, int voff) { __builtin_amdgcn_raw_buffer_store_b64(v, rs, voff, 0, AUX_SC1); }
#define CS3E 8
#define G2WORD SY_GO2
#define GATEN 60
#define CODEPF 1408
#define E1 2
#define E3 0
#define PFD 6
#define STAGED 1
#define XP 10
#define E4 0
#define XW 8
#define DEPTH 12
struct Loader {
  unsigned vsl  , vs6  , vs, cons; int pub;
#define LOOK "ds_read_b32 %3, %4 offset:%7\n\ts_waitcnt lgkmcnt(0)\n\t" \
             "v_min_u32_dpp %3, %3, %3 row_shr:2 row_mask:0xf bank_mask:0xf\n\ts_nop 1\n\t" \
             "v_min_u32_dpp %3, %3, %3 row_shr:1 row_mask:0xf bank_mask:0xf\n\ts_nop 0\n\t" \
             "v_readlane_b32 %0, %3, 3\n\ts_cmp_ge_u32 %0, %5\n\ts_cbranch_scc1 .Lback%=\n\t"
#define RUNG(N) "s_waitcnt vmcnt(" #N ")\n\ts_sub_i32 %1, %8, 2 * " #N "\n\ts_max_i32 %2, %2, %1\n\tv_mov_b32 %3, %2\n\tds_write_b32 %6, %3\n\t" LOOK
  unsigned tmp;
  DI void room(unsigned pos, unsigned end) {
    unsigned cnt;
    asm volatile("s_cmp_ge_i32 %0, %5\n\ts_cbranch_scc0 .Lslow%=\n.Lback%=:\n\t.subsection 1\n.Lslow%=:\n\t" LOOK RUNG(8) RUNG(4)
                 "s_waitcnt vmcnt(0)\n\tv_mov_b32 %3, %8\n\ts_mov_b32 %2, %8\n\tds_write_b32 %6, %3\n\ts_mov_b32 %1, 0x40000\n.Lsp%=:\n\t" LOOK
                 "s_sleep " STR(SLPL) "\n\ts_sub_u32 %1, %1, 1\n\ts_cbranch_scc0 .Lsp%=\n\ts_branch .Lback%=\n\t.subsection 0"
                 : "+s"(cons), "=&s"(cnt), "+s"(pub) : "v"(tmp), "v"(vs6), "s"(end - NR), "v"(vsl), "i"(SY_CONS - SY_OFF), "s"(pos) : "memory", "scc");
  }
  unsigned hv, rv;
  DI void look() { asm volatile("ds_read_b32 %0, %2 offset:%3\n\tds_read_b32 %1, %2 offset:%4" : "=&v"(hv), "=&v"(rv) : "v"(vs), "i"(SY_HOLD - SY_OFF), "i"(SY_READY - SY_OFF) : "memory"); }
  DI void done(unsigned end) {
    pub = max((int)end - 2 * DEPTH, pub);
    unsigned cnt;
    asm volatile("s_waitcnt vmcnt(%7) lgkmcnt(0)\n\tds_write_b32 %4, %5\n\tv_cmp_gt_u32 vcc, %2, %3\n\ts_cbranch_vccnz .Lslow%=\n.Lback%=:\n\t.subsection 1\n.Lslow%=:\n\t"
                 "s_waitcnt vmcnt(0)\n\tv_mov_b32 %2, %6\n\ts_mov_b32 %0, %6\n\tds_write_b32 %4, %2\n\ts_mov_b32 %1, 0x40000\n.Lsp%=:\n\t"
                 "ds_read_b32 %2, %8 offset:%9\n\tds_read_b32 %3, %8 offset:%10\n\ts_waitcnt lgkmcnt(0)\n\tv_cmp_gt_u32 vcc, %2, %3\n\ts_cbranch_vccz .Lback%=\n\t"
                 "s_sub_u32 %1, %1, 1\n\ts_cbranch_scc0 .Lsp%=\n\t"
                 "s_branch .Lback%=\n\t.subsection 0"
                 : "+s"(pub), "=&s"(cnt) : "v"(hv), "v"(rv), "v"(vsl), "v"(pub), "s"(end), "i"(DEPTH), "v"(vs), "i"(SY_HOLD - SY_OFF), "i"(SY_READY - SY_OFF) : "memory", "scc", "vcc");
  }
};
#define GL0(v, rs, so, c) "buffer_load_dwordx4 " v ", " rs ", " so " offen" c " lds\n\t"
#define GL1(v, rs, so, c) "s_nop 0\n\t" GL0(v, rs, so, c)
#define GLN(v, rs, so, c) "s_add_u32 m0, m0, 0x800\n\t" GL1(v, rs, so, c)
DI v4u srd(const void* base, unsigned bytes) { const u64 a = (u64)base; return v4u{(unsigned)a, (unsigned)(a >> 32) & 0xFFFFu, bytes, 0x00020000u}; }
DI void dma1(unsigned lds, unsigned voff, v4u r, unsigned so) { asm volatile("s_mov_b32 m0, %0\n\t" GL1("%1", "%2", "%3", "") :: "s"(lds), "v"(voff), "s"(r), "s"(so) : "memory"); }
DI int wg_of(int tt, int jj) { return ((tt & 3) * 8 + (jj & 7)) * 8 + (tt >> 2) * 2 + (jj >> 3); }
DI void frag_out(s16x8 f, __amdgpu_buffer_rsrc_t rs, int off, float& cs) {
  const v4u v = __builtin_bit_cast(v4u, f); const unsigned ones = 0x3f803f80u;
  stx(v, rs, off);
  cs = dot2(v.w, ones, dot2(v.z, ones, dot2(v.y, ones, dot2(v.x, ones, cs))));
}
constexpr int STG1 = 96 * 1024, STG2 = 104 * 1024, STG4 = 112 * 1024, STG3 = 120 * 1024;

DI void prep3(const Params& p, LAS char* L, unsigned lb, unsigned vs, __amdgpu_buffer_rsrc_t rs, int lane, int t, int j, int l) {
  const int h = lane >> 5, gi = (lane >> 4) & 1, q = (lane & 15) >> 2, pp = lane & 3, us = (j / 3) * 16 + (j % 3) * 6, ks = 2 * t + l;
  LAS char* bE = L + STG3 + l * 6144 + (4 * h + q) * 192 + 8 * pp + gi * (3072 + 32); LAS char* bO = bE - gi * 64;
  const v4u rg = srd(p.wg, 512 * 2752), ru = srd(p.wu, 512 * 2752);
  spin_all_ge<SY_RAW>(vs + 4 * (lane & 3), 1u);
#pragma unroll
  for (int i = 0; i < 6; ++i) {
    const int e = 64 * (i % 3) + lane, r = (e * 43691) >> 19, c = e - 12 * r, up = i / 3;
    dma1(lb + STG3 + (6 * l + i) * 1024, r * 2752 + (c ^ (2 * up)) * 16 + 32 * us, up ? ru : rg, ks * (16 * 2752));
  }
  DRAIN();
  s16x8 f[6];
#pragma unroll
  for (int u = 0; u < 6; ++u) { LAS char* b = ((u & 1) ? bO : bE) + 32 * u; f[u] = cat(trr(b), trr(b + 1536)); }
  sig<SY_SR>(vs + 4 * l, 1u);
  float cs[6];
#pragma unroll
  for (int u = 0; u < 6; ++u) { cs[u] = 0.f; frag_out(f[u], rs, WT3_OFF + (j * 192 + 6 * ks + u) * 1024 + lane * 16, cs[u]); }
#pragma unroll
  for (int u = 0; u < 6; u += 2) {
    auto r = __builtin_amdgcn_permlane32_swap(asu(cs[u]), asu(cs[u + 1]), false, false);
    __builtin_amdgcn_raw_buffer_store_b32(asu(asf(r[0]) + asf(r[1])), rs, CSP_OFF + ((j * 32 + ks) * 288 + 96 + 32 * u + lane) * 4, 0, AUX_SC1);
  }
  DRAIN();
}

DI void loader_main(const Params& p, LAS char* L, unsigned lb, int lane, int t, int j, int l, int wave) {
  const __amdgpu_buffer_rsrc_t rs = __builtin_amdgcn_make_buffer_rsrc((void*)p.ws, 0, WS_BYTES, 0x00020000);
  Loader ld; ld.vsl = lane == 0 ? lb + SY_LANDED + 4 * l : lb + SY_DUMMY + 4 * lane; ld.vs6 = lb + SY_OFF + 4 * (lane & 3);
  ld.cons = opq(0); ld.pub = opq(0); ld.vs = lb + SY_OFF; asm volatile("v_mov_b32 %0, 0" : "=v"(ld.tmp));
  auto fresh = [&]() { int x = lane; asm volatile("" : "+v"(x)); return x; };
  const int jc = j >> 1, idx = 2 * t + (j & 1);
  auto off2 = [&]() { const int x = fresh(); return (unsigned)((x >> 3) * 1024 + (((x & 7) ^ (((x >> 4) & 1) * 4)) * 16) + 128 * jc); };
  auto rd2 = [&](int stg, int c) {
    const int x = fresh(), h = x >> 5, gi = (x >> 4) & 1, q = (x & 15) >> 2, pp = x & 3;
    LAS char* b = L + stg + (((4 * h + q) * 128 + ((2 * gi + (pp >> 1)) ^ (4 * ((q >> 1) & 1))) * 16 + 8 * (pp & 1)) ^ (64 * c));
    return __builtin_bit_cast(v4u, cat(trr(b), trr(b + 1024)));
  };
  if (l == 0) {
    const v4u ro = srd(p.wo, 512 * 1024); const unsigned o2 = off2();
    dma1(lb + STG2, o2, ro, idx * 16384); dma1(lb + STG2 + 1024, o2 + 8192, ro, idx * 16384);
    DRAIN();
#pragma unroll
    for (int c = 0; c < 2; ++c) stx(rd2(STG2, c), rs, WT2_OFF + (jc * 64 + 4 * (idx & 15) + 2 * (idx >> 4) + c) * 1024 + lane * 16);
    DRAIN(); sig<SY_T2>(ld.vs, 1u);
  }
  const v4u rw = srd(p.ws, WS_BYTES);
  const unsigned lbl = lb + l * 1024;
  auto seg = [&](unsigned so, unsigned pos, int n) {
    const unsigned v0 = fresh() * 16 + l * 1024;
    unsigned m = lbl + (pos % NR) * 1024;
#pragma nounroll
    for (int i = 0; i < n; ++i) {
      ld.room(pos, pos + 8);
      ld.look();
      asm volatile("s_mov_b32 m0, %0\n\t" GL1("%1", "%5", "%6", " sc1") GLN("%2", "%5", "%6", " sc1") GLN("%3", "%5", "%6", " sc1") GLN("%4", "%5", "%6", " sc1")
                   :: "s"(m), "v"(v0), "v"(v0 + 2048), "v"(v0 + 4096), "v"(v0 + 6144), "s"(rw), "s"(so) : "memory", "scc");
      pos += 8; so += 8192; m += 8192; if (m >= lbl + NR * 1024) m -= NR * 1024;
      ld.done(pos);
    }
  };
  v4u w1[8][6];
  LAS char* L1; LAS char* L1b;
  auto base1 = [&]() { L1 = (LAS char*)(size_t)(lb + fresh() * 16 + l * 6144); L1b = opqv(L1 + 49152); };
  {
    spin_ge<SY_WT>(ld.vs, 1);
    STAMP(16);
    const unsigned v0 = fresh() * 16 + l * 1024;
    unsigned m = lbl, so = WT1_OFF + j * 96 * 1024;
#pragma nounroll
    for (int i = 0; i < 12; ++i) {
      asm volatile("s_mov_b32 m0, %0\n\t" GL1("%1", "%5", "%6", " sc1") GLN("%2", "%5", "%6", " sc1") GLN("%3", "%5", "%6", " sc1") GLN("%4", "%5", "%6", " sc1")
                   "s_waitcnt vmcnt(%7)\n\tds_write_b32 %8, %9"
                   :: "s"(m), "v"(v0), "v"(v0 + 2048), "v"(v0 + 4096), "v"(v0 + 6144), "s"(rw), "s"(so), "i"(DEPTH1), "v"(ld.vsl), "v"(max(8 * i + 8 - 2 * DEPTH1, 0)) : "memory", "scc");
      so += 8192; m += 8192;
    }
    STAMP(17);
  }
  {
    asm volatile("s_waitcnt vmcnt(12)" ::: "memory"); sigq(ld.vsl, 72);
    DRAIN(); sigq(ld.vsl, 73);
    STAMP(20);
    spin_all_ge<SY_LANDED>(ld.vs + 4 * (lane & 1), 73);
    base1();
    sfor<0, 8>([&](auto I) {
#pragma unroll
      for (int q = 0; q < 6; ++q) w1[I.v][q] = *(LAS v4u*)((I.v < 4 ? L1 : L1b) + (I.v & 3) * 12288 + q * 1024);
      SBAR();
    });
    ld.pub = opq(P_W2); sigq(ld.vsl, ld.pub);
    STAMP(21);
  }
  seg(WT2_OFF + jc * 64 * 1024, P_W2, 2);
  {
    const v4u rd = srd(p.wd, 1376 * 1024); const unsigned o2 = off2();
    spin_all_ge<SY_RAW>(ld.vs + 4 * (lane & 3), 1u);
#pragma unroll
    for (int i = 0; i < 4; ++i) if (i < 2 || l == 0) dma1(lb + STG4 + l * 4096 + i * 1024, o2 + (i & 1) * 8192, rd, (idx + 32 * (i < 2 ? l : 2)) * 16384);
    DRAIN(); ld.pub = opq(P_W2 + 16); sigq(ld.vsl, ld.pub);
    v4u f[4];
#pragma unroll
    for (int i = 0; i < 4; ++i) f[i] = rd2(STG4 + l * 4096 + (i >> 1) * 2048, i & 1);
    sig<SY_SR + 8>(ld.vs + 4 * l, 1u);
#pragma unroll
    for (int i = 0; i < 2; ++i) {
      const int u = idx + 32 * (i ? 2 : l), kh = u >= 43, r16 = u & 15, jp = 3 * (u >> 4) + (r16 >= 12 ? 2 : r16 >= 6), r = r16 - (r16 >= 12 ? 12 : (r16 >= 6 ? 6 : 0)), jr = jp - 8 * kh;
      const int a = (r < 3 && jr >= 0) ? 3 * jr + r : 24 + u - 43 * kh - 3 * (jr + 1);
      if (u < 86 && (i == 0 || l == 0)) {
#pragma unroll
        for (int c = 0; c < 2; ++c) stx(f[2 * i + c], rs, WT4_OFF + (jc * 172 + 4 * a + 2 * kh + c) * 1024 + lane * 16);
      }
    }
    DRAIN(); sig<SY_TC + 4>(ld.vs + 4 * l, 1u);
    STAMP(22);
    spin_all_ge<SY_SR>(ld.vs + 4 * (lane & 3), 1u);
  }
#pragma nounroll
  for (int pass = 0; pass < 2; ++pass) {
    const unsigned pb = pass * P_END;
    STAMP(40 + 8 * pass);
    seg(WT2_OFF + (jc * 64 + 16 - 16 * pass) * 1024, pb + P_W2 + 16 - 16 * pass, 6 + 2 * pass);
    STAMP(41 + 8 * pass);
    if (pass == 0) { DRAIN(); ld.pub = opq(P_W3); sigq(ld.vsl, ld.pub); spin_ge<SY_WT>(ld.vs, 2); }
    seg(WT3_OFF + j * 192 * 1024, pb + P_W3, 24);
    STAMP(42 + 8 * pass);
    seg(WT4_OFF + jc * 172 * 1024, pb + P_W4, T4 / 2);
    DRAIN(); ld.pub = opq(pb + P_END); sigq(ld.vsl, ld.pub);
    STAMP(43 + 8 * pass);
    if (pass == 1) break;
    base1();
    sfor<0, 8>([&](auto I) {
      const unsigned pos = P_END + 12 * I.v + 6 * l;
      ld.room(pos, pos + 6);
#pragma unroll
      for (int q = 0; q < 6; ++q) *(LAS v4u*)((I.v < 4 ? L1 : L1b) + (I.v & 3) * 12288 + q * 1024) = w1[I.v][q];
      ld.pub = opq(I.v < 7 ? pos + 12 : P_END + P_W2); sigq(ld.vsl, ld.pub);
    });
  }
}

struct Tail {
  v4u r[2 * (43 - T4)];
  DI void load(__amdgpu_buffer_rsrc_t rs, unsigned vs, int lane, int j, int e) {
    spin_ge<SY_WT>(vs, 2);
#pragma unroll
    for (int i = 0; i < 2 * (43 - T4); ++i) r[i] = ldx(rs, lane * 16 + (i & 1) * 1024, WT4_OFF + ((j >> 1) * 172 + 4 * (T4 + (i >> 1)) + 2 * e) * 1024);
    SBAR();
  }
  DI void put(LAS char* L, unsigned vs, int lane, int e, int pass) {
    spin_all_ge<SY_CONS, SLPS>(vs + 4 * (lane & 3), pass * P_END + pos4(43) - NR);
#pragma unroll
    for (int i = 0; i < 2 * (43 - T4); ++i) *(LAS v4u*)(L + slot_of(pos4(T4)) + (4 * (i >> 1) + (i & 1)) * 1024 + e * 2048 + lane * 16) = r[i];
    sig<SY_T4>(vs + 4 * e, pass + 1);
  }
};
DI void helper_main(const Params& p, LAS char* L, unsigned lb, unsigned vs, int lane, int t, int j) {
  const int wave = 5; (void)wave;
  const __amdgpu_buffer_rsrc_t rs = __builtin_amdgcn_make_buffer_rsrc((void*)p.ws, 0, WS_BYTES, 0x00020000);
  const int h = lane >> 5, gi = (lane >> 4) & 1, q = (lane & 15) >> 2, pp = lane & 3;
  {
    const v4u rq = srd(p.wqkv, 512 * 3072);
    const unsigned o1 = (lane >> 2) * 3072 + (lane & 3) * 16 + 64 * j;
#pragma unroll
    for (int i = 0; i < 6; ++i) dma1(lb + STG1 + i * 1024, o1 + (i % 3) * 1024, rq, (2 * t + i / 3) * (16 * 3072));
    const u64 e_ = __hip_atomic_load((GAS u64*)(p.ws + EPOCH_OFF), RLX_AGENT);
    asm volatile("s_waitcnt vmcnt(1)" ::: "memory");
    STAMP(12);
    LAS char* b1 = L + STG1 + (4 * h + q) * 64 + gi * 32 + pp * 8;
#pragma unroll
    for (int i = 0; i < 6; ++i) {
      const int ks = 2 * t + i / 3, c = (i % 3 + 2) % 3;
      const int pos = ks < QA ? 3 * ks + c : (c < 2 ? 3 * QA + 2 * (ks - QA) + c : 64 + ks);
      float cs = 0.f;
      frag_out(cat(trr(b1 + i * 1024), trr(b1 + i * 1024 + 512)), rs, WT1_OFF + (j * 96 + pos) * 1024 + lane * 16, cs);
      cs = sum32(cs);
      if (lane < 32) __builtin_amdgcn_raw_buffer_store_b32(asu(cs), rs, CSP_OFF + ((j * 32 + ks) * 288 + 32 * (i % 3) + lane) * 4, 0, AUX_SC1);
    }
    DRAIN();
    const u64 E = uni64(e_);
    GAS u64* mine = (GAS u64*)(p.ws + CSF_OFF + blockIdx.x * 256);
    spin_ge<SY_T2>(vs, 1u);
    if (lane == 0) __hip_atomic_store(mine, E + 1, RLX_AGENT);
    auto poll = [&](GAS u64* f, int n, u64 want) {
      __builtin_amdgcn_s_sleep(PREW);
      bool ok = lane >= n;
      for (unsigned k = 0; k < (1u << 16); ++k) {
        if (!ok) ok = __hip_atomic_load(f, RLX_AGENT) - want <= E + 2 - want;
        if (__all(ok)) break;
        if (GAPW) __builtin_amdgcn_s_sleep(GAPW);
      }
      __builtin_amdgcn_fence(__ATOMIC_ACQUIRE, "wavefront");
    };
    GAS u64* f = (GAS u64*)(p.ws + CSF_OFF + wg_of(lane & 15, j ^ ((lane >> 4) & 1)) * 256);
    poll(f, 32, E + 1);
    sig<SY_WT>(vs, 1u);
    STAMP(13);
    {
      const int l2 = lane < 48 ? lane : lane - 48, hi = l2 >= 24;
      v4u g[16];
#pragma unroll
      for (int i = 0; i < 16; ++i) g[i] = ldx(rs, hi * 1152 + (l2 - 24 * hi) * 16, CSP_OFF + (j * 32 + 2 * i) * 1152);
      SBAR();
      f32x4 tot = __builtin_bit_cast(f32x4, g[0]);
#pragma unroll
      for (int i = 1; i < 16; ++i) tot += __builtin_bit_cast(f32x4, g[i]);
#pragma unroll
      for (int i = 0; i < 4; ++i) tot[i] += asf(__builtin_amdgcn_ds_bpermute((lane + 24) * 4, asu(tot[i])));
      if (lane < 24) *(LAS f32x4*)(L + CS1_OFF + lane * 16) = tot;
      sig<SY_CS>(vs, 1u);
      STAMP(15);
    }
    prep3(p, L, lb, vs, rs, lane, t, j, 1);
    spin_all_ge<SY_TC>(vs + 4 * min(lane & 3, 2), 1u);
    STAMP(17);
    if (lane == 0) __hip_atomic_store(mine, E + 2, RLX_AGENT);
    poll(f, 32, E + 2);
    sig<SY_WT>(vs, 2u);
    STAMP(14);
    {
      f32x4 tot = {0.f, 0.f, 0.f, 0.f};
#pragma unroll
      for (int k = 0; k < 2; ++k) {
        v4u g[16];
#pragma unroll
        for (int i = 0; i < 16; ++i) g[i] = ldx(rs, 384 + min(lane, 47) * 16, CSP_OFF + (j * 32 + 16 * k + i) * 1152);
        SBAR();
#pragma unroll
        for (int i = 0; i < 16; ++i) tot += __builtin_bit_cast(f32x4, g[i]);
      }
      if (lane < 48) *(LAS f32x4*)(L + CS3_OFF + lane * 16) = tot;
      sig<SY_CS>(vs, 2u);
    }
  }
  spin_ge<SY_READY, SLPS>(vs, 3u);
  Tail tl; tl.load(rs, vs, lane, j, 1);
  tl.put(L, vs, lane, 1, 0);
  tl.put(L, vs, lane, 1, 1);
}

DI void poller_main(const Params& p, LAS char* L, unsigned vs, int lane, int t, int j, int wave) {
  const __amdgpu_buffer_rsrc_t rs = __builtin_amdgcn_make_buffer_rsrc((void*)p.ws, 0, WS_BYTES, 0x00020000);
  const u64 E = uni64(__hip_atomic_load((GAS u64*)(p.ws + EPOCH_OFF), RLX_AGENT));
  const int own = wg_of(t, lane & 15), prev = wg_of(t > 0 ? t - 1 : 0, lane & 15);
  auto hop = [&](unsigned ev, bool halo) {
    spin_all_ge<SY_PROG, SLPP>(vs + 4 * (lane & 3), ev);
    if (lane == 0) __hip_atomic_store((GAS u64*)(p.ws + FLAG_OFF + blockIdx.x * 256), E + ev, RLX_AGENT);
    GAS u64* f = (GAS u64*)(p.ws + FLAG_OFF + ((halo && lane >= 16) ? prev : own) * 256);
    if (ev == 1) __builtin_amdgcn_s_sleep(PRE1); else if (ev % 5 == 4) __builtin_amdgcn_s_sleep(PREA); else if (ev % 5 == 0) __builtin_amdgcn_s_sleep(PREB); else
    __builtin_amdgcn_s_sleep(PRE);
    bool ok = lane >= (halo ? 32 : 16);
    for (unsigned n = 0; n < (1u << 16); ++n) {
      if (!ok || !MASKP) ok = lane >= (halo ? 32 : 16) || __hip_atomic_load(f, RLX_AGENT) - (E + ev) <= (u64)(10u - ev);
      if (__all(ok)) break;
      if (GAP) __builtin_amdgcn_s_sleep(GAP);
    }
    __builtin_amdgcn_fence(__ATOMIC_ACQUIRE, "wavefront");
    sig<SY_READY>(vs, ev);
  };
  Tail tl;
  sfor<0, 2>([&](auto PASS) {
    constexpr int pass = PASS.v;
    hop(1 + 5 * pass, true);
    STAMP(1 + 4 * pass);
    {
      const int hb = t > 0 ? 4 * t - 1 : 0, G = lane >> 4;
      const int hvo = (G >> 1) * 1024 + (16 + (lane & 15) + 32 * (G & 1)) * 16, xb = X_OFF + 2 * pass * X_SZ + hb * 32768;
      v4u a[4], hx[16];
#pragma unroll
      for (int i = 0; i < 4; ++i) a[i] = ldx(rs, lane * 16 + i * 1024, ST_OFF + 2 * pass * ST_SZ + hb * 4096);
#pragma unroll
      for (int i = 0; i < 16; ++i) hx[i] = ldx(rs, hvo + (i & 1) * 2048, xb + (i >> 1) * 4096);
      SBAR();
      const v4u r = ln_stats(a);
      if (lane >= 8 && lane < 16) *(LAS v4u*)(L + LN_OFF + (2 * lane - 16) * 8) = r;
#pragma unroll
      for (int i = 0; i < ((AB & 4) ? 1 : 16); ++i) *(LAS v4u*)(L + EX_OFF + i * 1024 + lane * 16) = hx[i];
      sig<SY_LN>(vs, pass + 1);
    }
    if constexpr (pass == 0) { prep3(p, L, (unsigned)(size_t)L, vs, rs, lane, t, j, 0); sig<SY_TC>(vs, 1u); }
#pragma nounroll
    for (unsigned e = 2; e <= 5; ++e) {
      hop(e + 5 * pass, false);
      if (e != 5) STAMP(e + 4 * pass); else STAMP(62 + pass);
      if (pass == 0 && e == 3) tl.load(rs, vs, lane, j, 0);
    }
    tl.put(L, vs, lane, 0, pass);
  });
  if (blockIdx.x == 0) {
    for (unsigned n = 0; n < (1u << 16); ++n) {
      bool ok = true;
#pragma unroll
      for (int i = 0; i < 4; ++i) ok &= (__hip_atomic_load((GAS u64*)(p.ws + FLAG_OFF + (i * 64 + lane) * 256), RLX_AGENT) - (E + 2) <= 8ull);
      if (__all(ok)) break;
    }
    if (lane == 0) __hip_atomic_store((GAS u64*)(p.ws + EPOCH_OFF), E + 16, RLX_AGENT);
  }
}

template <int S> DI v4u ldf(__amdgpu_buffer_rsrc_t rs, int voff, int base) { return ldx(rs, voff + (S & 3) * 1024, base + (S >> 2) * 4096); }
struct LB { LAS char* b[3]; DI explicit LB(LAS char* x) { b[0] = opqv(x); b[1] = opqv(x + 60 * 1024); b[2] = opqv(x + 120 * 1024); }
  template <int OFF> DI LAS char* at() const { return b[OFF / (60 * 1024)] + OFF % (60 * 1024); } };

#define HOLDMASK 0
template <int PH> DI void hold(unsigned vs, unsigned ev) { if constexpr ((HOLDMASK >> PH) & 1) sig<SY_HOLD>(vs, ev); }
struct Resid { v4u a, b; };
DI Resid ld_resid(__amdgpu_buffer_rsrc_t rs, int lane, int wave, int t, int j, int r_off) {
  const int ro = opq(r_off + ((4 * t + 2 * (j & 1) + (wave & 1)) * 32 + 4 * (j >> 1) + 2 * (wave >> 1)) * 1024);
  return Resid{ldf<0>(rs, lane * 16, ro), ldf<1>(rs, lane * 16, ro)};
}
template <int NKS, bool IS_DOWN>
DI void proj_phase(const Params& p, LAS char* L, unsigned vs, __amdgpu_buffer_rsrc_t rs, int lane, int wave, int t, int j, int pass, unsigned& have,
                   int a_off  , const Resid& res, int x_off, int st_off, unsigned ev  , unsigned ph) {
  const int m2 = wave & 1, kh = wave >> 1, th = j & 1, jc = j >> 1, tb = 4 * t + 2 * th + m2;
  const int h = lane >> 5;
  const unsigned pb = pass * P_END, vsw = vs + 4 * wave;
  const unsigned vq = lane == 0 ? vsw + (SY_CONS - SY_OFF) : vs + (SY_DUMMY - SY_OFF) + 4 * lane;
  f32x16 acc[2];
#pragma unroll
  for (int i = 0; i < 4; ++i) { acc[0][2 * i] = bflo(res.a[i]); acc[0][2 * i + 1] = bfhi(res.a[i]); acc[0][8 + 2 * i] = bflo(res.b[i]); acc[0][9 + 2 * i] = bfhi(res.b[i]); }
#pragma unroll
  for (int i = 0; i < 16; ++i) acc[1][i] = 0.f;
  asm volatile("" : "+v"(acc[0]));
  auto posk = [](int a) { return IS_DOWN ? pos4(a) : pos2(a); };
  const LB b0(L + lane * 16 + kh * 3072), b1(L + lane * 16 + kh * 1024 + 1024);
  auto rd = [&](auto N) -> s16x8 {
    constexpr int a = N.v >> 1, c = N.v & 1, sl = slot_of(IS_DOWN ? pos4(a) : pos2(a));
    const LB& b = c ? b1 : b0;
    return *(LAS s16x8*)b.template at<sl>();
  };
  constexpr int NOP = 2 * NKS, PF = PFD, LAST = IS_DOWN ? T4 : NKS;
  s16x8 wf[PF];
  wait_landed(vs, have, pb + posk(8));
  sfor<0, PF>([&](auto N) { wf[N.v] = rd(N); });
  const int ab = opq(a_off + tb * (IS_DOWN ? 86 : 32) * 1024 + (IS_DOWN ? 0 : kh * 16384)), khk = opq(kh * 1024);
  v4u xf[NKS];
  auto ldk = [&](auto K) {
    constexpr int u0 = ord4(0, K.v), u1 = ord4(1, K.v);
    if constexpr (IS_DOWN) xf[K.v] = ldx(rs, lane * 16, ab + u0 * 1024 + khk * (u1 - u0));
    else xf[K.v] = ldf<K.v>(rs, lane * 16, ab);
  };
  SBAR();
  spin_ge<SY_READY, SLPC>(vs, ev + (IS_DOWN && !STAGED ? 1 : 0));
  STAMP(16 + 4 * ph);
  CDECL(); if (IS_DOWN) CST(4, 0);
  sfor<0, XP>([&](auto S) { ldk(S); });
  SBAR();
  v2u pl; unsigned pf;
  sfor<0, NOP>([&](auto N) {
    constexpr int n = N.v;
    const s16x8 cur = wf[n % PF];
    if constexpr (n + PF < NOP) {
      constexpr int a2 = (n + PF) >> 1;
      if constexpr (IS_DOWN && n + PF == 18) sigq(vq, pb + posk(9));
      if constexpr (((n + PF) & 15) == 10 && a2 + 3 < LAST) pl = peek_landed(vs);
      if constexpr (((n + PF) & 15) == 15 && a2 + 1 < LAST) take_landed(have, pl);
      if constexpr (((n + PF) & 15) == 0 && a2 < LAST) wait_landed(vs, have, pb + posk(a2 + 8 < LAST ? a2 + 8 : LAST));
      if constexpr (IS_DOWN && n + PF == 2 * T4 - 5) pf = peek(vs + 4 * (lane & 1), SY_T4);
      if constexpr (IS_DOWN && n + PF == 2 * T4 - 1) need_all<SY_T4>(pf, vs + 4 * (lane & 1), pass + 1);
      wf[n % PF] = rd(IC<n + PF>{});
    }
    acc[n & 1] = mfma32(cur, xf[n >> 1], acc[n & 1]);
    constexpr int NB = 2 * (24 - XP - E4) + 1;
    if constexpr (IS_DOWN && STAGED && n == NB - 6) pf = peek(vs, SY_READY);
    if constexpr (IS_DOWN && STAGED && n == NB - 1) need_all<SY_READY>(pf, vs, ev + 1);
    constexpr int k = n < 2 * E4 ? XP + n : ((n & 1) ? XP + E4 + (n >> 1) : NKS);
    if constexpr (k < NKS) ldk(IC<k>{});
    if constexpr (IS_DOWN) { if constexpr (n == 1) CST(4, 1); if constexpr (n == 15) CST(4, 2); if constexpr (n == 31) CST(4, 3); if constexpr (n == 47) CST(4, 4); if constexpr (n == 63) CST(4, 5); if constexpr (n == 75) CST(4, 6); if constexpr (n == 85) CST(4, 7); }
    SBAR();
  });
  asm volatile("" : "+v"(acc[0]), "+v"(acc[1]));
  sigq(vq, pb + (IS_DOWN ? P_END : posk(NKS)));
  const unsigned evo = ev + (IS_DOWN ? 2 : 1);
  if (!(IS_DOWN && pass == 1)) hold<IS_DOWN ? 4 : 2>(vs, evo);
  if (IS_DOWN) CDUMP(4);
  STAMP(17 + 4 * ph);
#pragma unroll
  for (int g = 0; g < 4; ++g) *(LAS f32x4*)(L + EX_OFF + wave * 4096 + g * 1024 + lane * 16) = f32x4{acc[1][4 * g], acc[1][4 * g + 1], acc[1][4 * g + 2], acc[1][4 * g + 3]};
  sig<SY_XF>(vsw, evo);
  spin_ge<SY_XF>(vs + 4 * (wave ^ 2), evo);
  unsigned o[8]; f32x2 xv[8];
#pragma unroll
  for (int g = 0; g < 4; ++g) {
    const f32x4 e = *(LAS f32x4*)(L + EX_OFF + (wave ^ 2) * 4096 + g * 1024 + lane * 16);
    xv[2 * g] = f32x2{acc[0][4 * g], acc[0][4 * g + 1]} + f32x2{e[0], e[1]};
    xv[2 * g + 1] = f32x2{acc[0][4 * g + 2], acc[0][4 * g + 3]} + f32x2{e[2], e[3]};
    o[2 * g] = pk(xv[2 * g]); o[2 * g + 1] = pk(xv[2 * g + 1]);
  }
  if (IS_DOWN && pass == 1) {
    GAS char* dst = (GAS char*)p.out + (size_t)(tb * 32 + (lane & 31)) * 1024 + (64 * jc + 32 * kh + 4 * h) * 2;
#pragma unroll
    for (int g = 0; g < 4; ++g) *(GAS v2u*)(dst + 16 * g) = v2u{o[2 * g], o[2 * g + 1]};
    STAMP(18 + 4 * ph);
    return;
  }
  const int xo = x_off + (tb * 32 + 4 * jc + 2 * kh) * 1024 + lane * 16;
  stx(v4u{o[0], o[1], o[2], o[3]}, rs, xo);
  stx(v4u{o[4], o[5], o[6], o[7]}, rs, xo + 1024);
  const f32x2 s2 = ((xv[0] + xv[1]) + (xv[2] + xv[3])) + ((xv[4] + xv[5]) + (xv[6] + xv[7]));
  const float sum = sum32(s2.x + s2.y);
  const f32x2 mi = sp(sum * (-1.f / 32.f));
  f32x2 q0 = {0.f, 0.f}, q1 = {0.f, 0.f};
#pragma unroll
  for (int i = 0; i < 4; ++i) { const f32x2 d0 = xv[2 * i] + mi, d1 = xv[2 * i + 1] + mi; q0 = fma2(d0, d0, q0); q1 = fma2(d1, d1, q1); }
  q0 += q1;
  const float m2s = sum32(q0.x + q0.y);
  if (lane < 32) stx2(v2u{asu(sum), asu(m2s)}, rs, lane * 8 + st_off + (tb * 16 + 2 * jc + kh) * 256);
  STAMP(18 + 4 * ph);
  publish(vsw, evo);
  STAMP(19 + 4 * ph);
}

DI void compute_main(const Params& p, LAS char* L, unsigned vs, int lane, int wave, int t, int j) {
  const __amdgpu_buffer_rsrc_t rs = __builtin_amdgcn_make_buffer_rsrc((void*)p.ws, 0, WS_BYTES, 0x00020000);
  unsigned have = 0;
  const unsigned vsw = vs + 4 * wave;
  STAMP(10);
  {
    const int c = lane >> 3, tk = lane & 7, odd = c & 1, blk = 4 * t + (j >> 2), sl = 8 * (j & 3) + tk;
    const GAS char* src = (const GAS char*)p.x + (size_t)(128 * t + 8 * j + tk) * 1024 + (16 * wave + c) * 16;
    v4u d[2];
    d[0] = *(const GAS v4u*)src; d[1] = *(const GAS v4u*)(src + 128);
    SBAR();
    STAMP(14);
#pragma unroll
    for (int a = 0; a < 2; ++a) {
      const int cI = 16 * wave + 8 * a + c;
      const unsigned r0 = ror8(odd ? d[a].x : d[a].z), r1 = ror8(odd ? d[a].y : d[a].w);
      const v4u o = odd ? v4u{r0, r1, d[a].z, d[a].w} : v4u{d[a].x, d[a].y, r0, r1};
      stx(o, rs, (cI >> 1) * 1024 + (sl + 32 * odd) * 16 + X_OFF + blk * 32768);
      float xv[8], s = 0.f;
#pragma unroll
      for (int i = 0; i < 4; ++i) { xv[2 * i] = bflo(d[a][i]); xv[2 * i + 1] = bfhi(d[a][i]); s += xv[2 * i] + xv[2 * i + 1]; }
      s += asf(ror8(asu(s))); s = sum16(s);
      const float mi = s * (1.f / 32.f); float m2 = 0.f;
#pragma unroll
      for (int i = 0; i < 8; ++i) { const float e = xv[i] - mi; m2 += e * e; }
      m2 += asf(ror8(asu(m2))); m2 = sum16(m2);
      if ((c & 3) == 0) stx2(v2u{asu(s), asu(m2)}, rs, (cI >> 2) * 256 + sl * 8 + ST_OFF + blk * 4096);
    }
    hold<0>(vs, 1);
    STAMP(11);
    publish(vsw, 1);
    STAMP(12);
  }
#pragma nounroll
  for (int pass = 0; pass < 2; ++pass) {
    const unsigned pb = pass * P_END, ph = 4 * pass + 1, eb = 5 * pass + 1;
    Resid res;
    const int xin = X_OFF + 2 * pass * X_SZ, stin = ST_OFF + 2 * pass * ST_SZ;
    const int tb = 4 * t + wave;
    {
      int ln = lane; asm volatile("" : "+v"(ln));
      const int h = ln >> 5, tok = ln & 31;
      CDECL();
      v4u xf[32], hx[16]; LnSteps lns;
      const int xb = opq(xin + tb * 32768), sb = opq(stin + tb * 4096);
      const int G = ln >> 4;
      const LB b1(L + ln * 16);
      f32x16 acc[3];
#pragma unroll
      for (int c = 0; c < 3; ++c)
#pragma unroll
        for (int i = 0; i < 16; ++i) acc[c][i] = 0.f;
      constexpr int PF = PFD;
      s16x8 wf[PF];
      const int oh = (16 * (wave >> 1) + (ln & 15) + 32 * (G & 1)) * 16 + (wave & 1) * 1024;
      const LB bh(L + oh + (G >> 1) * 3072);
      LAS char* bh2 = opqv(L + oh + (G >> 1) * 2048 + 3 * QA * 1024);
      auto rd = [&](auto N) -> s16x8 {
        constexpr int n = N.v;
        if constexpr (n >= 88 && n < 88 + QA / 2) return *(LAS s16x8*)bh.at<(n - 88) * 6144>();
        else if constexpr (n >= 88 && n < 104) return *(LAS s16x8*)(bh2 + (n - 88 - QA / 2) * 4096);
        else { constexpr int o = (n < 88 ? n : n - 16) * 1024; return *(LAS s16x8*)b1.at<o>(); }
      };
      f32x4 hacc = {0.f, 0.f, 0.f, 0.f};
      const unsigned vq = ln == 0 ? vsw + (SY_CONS - SY_OFF) : vs + (SY_DUMMY - SY_OFF) + 4 * ln, vs4 = vs + 4 * (ln & 3);
      LAS char* csb = opqv(L + CS1_OFF + 16 * h);
      LAS char* vw = opqv(L + EX_OFF + 2 * h * PLANE + (16 + 32 * wave + tok) * 16);
      LAS char* kw = opqv(L + KP_OFF + h * PLANE + (16 + 32 * wave + tok) * 16);
      LAS char* hxb = opqv(L + EX_OFF + ln * 16);
      wait_landed(vs, have, pb + 24);
      sfor<0, PF>([&](auto N) { wf[N.v] = rd(N); });
      SBAR();
      spin_ge<SY_READY, SLPC>(vs, eb);
      STAMP(16 + 4 * ph);
      CST(5, 0);
      v2u pl; unsigned pf, pc; f32x4 cs[12]; f32x4 csh; f32x2 lnh;
      sfor<0, XW>([&](auto S) { xf[S.v] = ldf<S.v>(rs, ln * 16, xb); });
      SBAR();
      CST(5, 1);
      auto op = [&](auto N) {
        constexpr int n = N.v, r = n + PF;
        const s16x8 cur = wf[n % PF];
        if constexpr (r < 112) wf[n % PF] = rd(IC<r>{});
        constexpr int st = n < 3 * QA ? n / 3 : QA + (n - 3 * QA) / 2, c = n < 3 * QA ? (n % 3 + 1) % 3 : 1 + (n - 3 * QA) % 2;
        if constexpr (n < 88) acc[c] = mfma32(cur, xf[st], acc[c]);
        else if constexpr (n < 104) hacc = mfma16(cur, hx[n - 88], hacc);
        else acc[0] = mfma32(cur, xf[n - 80], acc[0]);
      };
      sfor<0, 104>([&](auto N) {
        constexpr int n = N.v, r = n + PF;
        if constexpr (r % 24 == 17 && r < 72) pl = peek_landed(vs);
        if constexpr (r % 24 == 23 && r < 72) take_landed(have, pl);
        if constexpr (r % 24 == 0 && r < 96) wait_landed(vs, have, pb + r + 24);
        op(N);
        {
          constexpr int st_ = n / 3, k = n % 3 == 0 ? XW + st_ + (st_ < E1 ? st_ : E1) : (n % 3 == 1 && st_ < E1 ? XW + 2 * st_ + 1 : 32);
          if constexpr (k < 32) xf[k] = ldf<k>(rs, ln * 16, xb);
          if constexpr (n % 3 == 2 && n / 3 >= 4 && n / 3 < 12) lns.st[n / 3 - 4] = ldx2(rs, h * 2048 + tok * 8 + (n / 3 - 4) * 256, sb);
        }
        if constexpr (n == GATEN) sig<SY_RAW>(vsw, 1u);
        if constexpr (n >= 50 && n < 50 + LnSteps::N) lns.template step<n - 50>();
        if constexpr (n == 74) { pf = peek(vs, SY_LN); pc = peek(vs, SY_CS); }
        if constexpr (n == 79) need_all<SY_LN>(pf, vs, pass + 1);
        if constexpr (n == 81) need_all<SY_CS>(pc, vs, 1u);
        if constexpr (n >= 84 && n < 100) hx[n - 84] = *(LAS v4u*)(hxb + (n - 84) * 1024);
        static_assert(100 + PF >= 103, "");
        if constexpr (n == 100) sigq(vq, pb + KP_OFF / 1024);
        if constexpr (n >= 100 && n < 104) cs[n - 100] = *(LAS f32x4*)(csb + (32 + 8 * (n - 100)) * 4);
        if constexpr (n == 2) CST(5, 2); if constexpr (n == 23) CST(5, 3); if constexpr (n == 47) CST(5, 4); if constexpr (n == 71) CST(5, 5); if constexpr (n == 87) CST(5, 6); if constexpr (n == 103) CST(5, 7);
        SBAR();
      });
      asm volatile("" : "+v"(acc[1]), "+v"(acc[2]), "+v"(hacc));
      STAMP(17 + 4 * ph);
      CST(1, 0);
      const f32x2 rstd = sp(lns.rstd), nmean = sp(lns.nmean);
      {
        unsigned pk_[8]; f32x2 vv[8];
        op(IC<104>{});
#pragma unroll
        for (int i = 4; i < 8; ++i) cs[i] = *(LAS f32x4*)(csb + (32 + 8 * i) * 4);
        csh = *(LAS f32x4*)(L + CS1_OFF + (32 * (1 + (wave & 1)) + 16 * (wave >> 1) + 4 * G) * 4); lnh = *(LAS f32x2*)(L + LN_OFF + (ln & 15) * 8);
        SBAR(); op(IC<105>{}); SBAR();
        sfor<0, 8>([&](auto I) { constexpr int i = I.v; pk_[i] = pk(fma2(f32x2{cs[i >> 1][2 * (i & 1)], cs[i >> 1][2 * (i & 1) + 1]}, nmean, pr<i>(acc[1])) * rstd); });
        pf = peek(vs4, SY_CONS);
        SBAR(); op(IC<106>{}); op(IC<107>{}); SBAR();
        sfor<0, 8>([&](auto I) { constexpr int i = I.v; vv[i] = fma2(f32x2{cs[4 + (i >> 1)][2 * (i & 1)], cs[4 + (i >> 1)][2 * (i & 1) + 1]}, nmean, pr<i>(acc[2])) * rstd; });
        const f32x2 hr = sp(t == 0 ? 0.f : lnh.x), hm = sp(t == 0 ? 0.f : lnh.y);
        f32x2 hv0 = fma2(f32x2{csh[0], csh[1]}, hm, f32x2{hacc[0], hacc[1]}) * hr, hv1 = fma2(f32x2{csh[2], csh[3]}, hm, f32x2{hacc[2], hacc[3]}) * hr;
        if (t == 0) { hv0 = sp(0.f); hv1 = sp(0.f); }
        SBAR();
        CST(1, 1);
        need_all<SY_CONS>(pf, vs4, pb + KP_OFF / 1024);
        op(IC<108>{});
        *(LAS v4u*)kw = v4u{pk_[0], pk_[1], pk_[2], pk_[3]};
        *(LAS v4u*)(kw + 2 * PLANE) = v4u{pk_[4], pk_[5], pk_[6], pk_[7]};
        SBAR(); op(IC<109>{}); SBAR();
        if (wave & 1) *(LAS f32x4*)(L + EX_OFF + ((wave >> 1) * 4 + 2 * (G & 1) + (G >> 1)) * PLANE + (ln & 15) * 16) = f32x4{hv0.x, hv0.y, hv1.x, hv1.y};
        else *(LAS v2u*)(L + KP_OFF + ((wave >> 1) * 2 + (G & 1)) * PLANE + (ln & 15) * 16 + 8 * (G >> 1)) = v2u{pk(hv0), pk(hv1)};
        sig<SY_KV>(vsw, 2 * pass + 1);
        SBAR(); op(IC<110>{}); SBAR();
#pragma unroll
        for (int g = 0; g < 4; ++g) *(LAS f32x4*)(vw + ((g >> 1) * 4 + (g & 1)) * PLANE) = f32x4{vv[2 * g].x, vv[2 * g].y, vv[2 * g + 1].x, vv[2 * g + 1].y};
        sig<SY_KV>(vsw, 2 * pass + 2);
        SBAR(); op(IC<111>{});
#pragma unroll
        for (int i = 8; i < 12; ++i) cs[i] = *(LAS f32x4*)(csb + 8 * (i - 8) * 4);
        res = ld_resid(rs, ln, wave, t, j, xin);
        SBAR();
      }
      CST(1, 2);
      spin_all_ge<SY_KV>(vs4, 2 * pass + 1);
      CST(1, 3);
      LAS char* kb = L + KP_OFF + h * 2 * PLANE + (16 + 32 * wave + tok - 14) * 16;
      LAS char* vb = L + EX_OFF + h * 4 * PLANE + (16 + 32 * wave + tok - 14) * 16;
      float lg[8]; v4u kk[16]; f32x4 vr[32];
#pragma unroll
      for (int w = 0; w < 8; ++w) { kk[2 * w] = *(LAS v4u*)(kb + (14 - 2 * w) * 16); kk[2 * w + 1] = *(LAS v4u*)(kb + PLANE + (14 - 2 * w) * 16); }
      SBAR();
      unsigned qq[8];
      {
        unsigned pq[8];
        sfor<0, 8>([&](auto I) { constexpr int i = I.v; pq[i] = pk(fma2(f32x2{cs[8 + (i >> 1)][2 * (i & 1)], cs[8 + (i >> 1)][2 * (i & 1) + 1]}, nmean, pr<i>(acc[0]))); });
#pragma unroll
        for (int i = 0; i < 4; ++i) { auto r = __builtin_amdgcn_permlane32_swap(pq[i], pq[4 + i], false, false); qq[i] = r[0]; qq[4 + i] = r[1]; }
      }
      pf = peek(vs4, SY_KV);
      SBAR();
      CST(1, 4);
      need_all<SY_KV>(pf, vs4, 2 * pass + 2);
      sfor<0, 4>([&](auto W) {
        constexpr int w = 2 * W.v;
#pragma unroll
        for (int g = 0; g < 8; ++g) vr[4 * w + g] = *(LAS f32x4*)(vb + (g & 3) * PLANE + (14 - 2 * (w + (g >> 2))) * 16);
        float a = dot2(qq[0], kk[2 * w].x, 0.f), b = dot2(qq[0], kk[2 * w + 2].x, 0.f);
#pragma unroll
        for (int i = 1; i < 8; ++i) { a = dot2(qq[i], kk[2 * w + (i >> 2)][i & 3], a); b = dot2(qq[i], kk[2 * w + 2 + (i >> 2)][i & 3], b); }
        lg[w] = a; lg[w + 1] = b;
        SBAR();
      });
      asm volatile("" : "+v"(lg[0]), "+v"(lg[1]), "+v"(lg[2]), "+v"(lg[3]), "+v"(lg[4]), "+v"(lg[5]), "+v"(lg[6]), "+v"(lg[7]));
      sigq(vq, pb + P_W2);
      hold<1>(vs, eb + 1);
      CST(1, 6);
      if (t == 0 && wave == 0) {
#pragma unroll
        for (int w = 1; w < 8; ++w) lg[w] = tok >= 2 * w ? lg[w] : -1e30f;
      }
      const float mx = max3(max3(lg[0], lg[1], lg[2]), max3(lg[3], lg[4], lg[5]), max3(lg[6], lg[7], lg[7]));
      const float sc = lns.rstd * (0.25f * 1.4426950408889634f);
      f32x2 e2[4];
#pragma unroll
      for (int w = 0; w < 4; ++w) { const f32x2 z = (f32x2{lg[2 * w], lg[2 * w + 1]} - sp(mx)) * sp(sc); e2[w] = f32x2{__builtin_amdgcn_exp2f(z.x), __builtin_amdgcn_exp2f(z.y)}; }
      const f32x2 s2 = (e2[0] + e2[1]) + (e2[2] + e2[3]);
      const f32x2 inv = sp(__builtin_amdgcn_rcpf(s2.x + s2.y));
      f32x2 ov[8];
#pragma unroll
      for (int w = 0; w < 8; ++w) {
        const f32x2 pw = sp(e2[w >> 1][w & 1]);
#pragma unroll
        for (int g = 0; g < 4; ++g) {
          const f32x4 v = vr[4 * w + g];
          ov[2 * g] = w ? fma2(f32x2{v[0], v[1]}, pw, ov[2 * g]) : f32x2{v[0], v[1]} * pw;
          ov[2 * g + 1] = w ? fma2(f32x2{v[2], v[3]}, pw, ov[2 * g + 1]) : f32x2{v[2], v[3]} * pw;
        }
      }
      const int oo = O_OFF + pass * X_SZ + (tb * 32 + 2 * j) * 1024 + h * 1024 + tok * 16;
      stx(v4u{pk(ov[0] * inv), pk(ov[1] * inv), pk(ov[2] * inv), pk(ov[3] * inv)}, rs, oo);
      stx(v4u{pk(ov[4] * inv), pk(ov[5] * inv), pk(ov[6] * inv), pk(ov[7] * inv)}, rs, oo + 512);
      CST(1, 7); CDUMP(1); CDUMP(5);
      STAMP(18 + 4 * ph);
      publish(vsw, eb + 1);
      STAMP(19 + 4 * ph);
    }
    { int ln = lane; asm volatile("" : "+v"(ln));
      proj_phase<16, false>(p, L, vs, rs, ln, wave, t, j, pass, have, O_OFF + pass * X_SZ, res, xin + X_SZ, stin + ST_SZ, eb + 1, ph + 1); }
    {
      int ln = lane; asm volatile("" : "+v"(ln));
      const int h = ln >> 5, tok = ln & 31;
      CDECL();
      v4u xf[32]; LnSteps lns;
      const int xb = opq(xin + X_SZ + tb * 32768), sb = opq(stin + ST_SZ + tb * 4096);
      const LB b3(L + ln * 16);
      f32x16 acc[6];
#pragma unroll
      for (int c = 0; c < 6; ++c)
#pragma unroll
        for (int i = 0; i < 16; ++i) acc[c][i] = 0.f;
      constexpr int PF = PFD;
      s16x8 wf[PF];
      auto rd = [&](auto N) -> s16x8 { return *(LAS s16x8*)b3.template at<slot_of(P_W3 + N.v)>(); };
      const unsigned vq = ln == 0 ? vsw + (SY_CONS - SY_OFF) : vs + (SY_DUMMY - SY_OFF) + 4 * ln;
      wait_landed(vs, have, pb + pos3(4));
      sfor<0, PF>([&](auto N) { wf[N.v] = rd(N); });
      SBAR();
      spin_ge<SY_READY, SLPC>(vs, eb + 2);
      v2u pl; unsigned pc;
      STAMP(16 + 4 * (ph + 2));
      CST(3, 0);
      sfor<0, XW>([&](auto S) { xf[S.v] = ldf<S.v>(rs, ln * 16, xb); });
      SBAR();
      sfor<0, 192>([&](auto N) {
        constexpr int n = N.v;
        const s16x8 cur = wf[n % PF];
        if constexpr (n + PF < 192) {
          if constexpr (((n + PF) % 12 == 2 && n + PF > 12) || (n + PF) == 188) sigq(vq, pb + pos3((n + PF) / 6));
          if constexpr (n == 100) pc = peek(vs, SY_CS);
          if constexpr (n == 106) need_all<SY_CS>(pc, vs, 2u);
          if constexpr (!(AB & 8) && (n + PF) % 24 == 14 && n + PF < 24 * 7) pl = peek_landed(vs);
          if constexpr (!(AB & 8) && (n + PF) % 24 == 20 && n + PF < 24 * 7) take_landed(have, pl);
          if constexpr (!(AB & 8) && (n + PF) % 24 == 22 && n + PF < 24 * 7) wait_landed(vs, have, pb + pos3((n + PF) / 6 + 5));
          wf[n % PF] = rd(IC<n + PF>{});
        }
        acc[n % 6] = mfma32(cur, xf[n / 6], acc[n % 6]);
        {
          constexpr int st_ = n / 6, k = n % 6 == 0 ? XW + st_ + (st_ < E3 ? st_ : E3) : (n % 6 == 4 && st_ < E3 ? XW + 2 * st_ + 1 : 32);
          if constexpr (k < 32) { if constexpr (AB & 1) xf[k] = xf[k % 8]; else xf[k] = ldf<k>(rs, ln * 16, xb); }
        }
        if constexpr (!(AB & 2) && n % 6 == 2 && n / 6 < 8) lns.st[n / 6] = ldx2(rs, h * 2048 + tok * 8 + (n / 6) * 256, sb);
        if constexpr (!(AB & 2) && n >= 90 && n % 3 == 0 && (n - 90) / 3 < LnSteps::N) lns.template step<(n - 90) / 3>();
        if constexpr (n == 5) CST(3, 1); if constexpr (n == 47) CST(3, 2); if constexpr (n == 95) CST(3, 3); if constexpr (n == 119) CST(3, 4); if constexpr (n == 143) CST(3, 5); if constexpr (n == 167) CST(3, 6); if constexpr (n == 191) CST(3, 7);
        SBAR();
      });
      asm volatile("" : "+v"(acc[0]), "+v"(acc[1]), "+v"(acc[2]), "+v"(acc[3]), "+v"(acc[4]), "+v"(acc[5]));
      sig<SY_CONS>(vsw, pb + P_W4);
      CDUMP(3);
      STAMP(17 + 4 * (ph + 2));
      const f32x2 k1 = sp(lns.rstd * -2.3022081985f), k3 = sp(lns.rstd * lns.rstd * lns.rstd * -0.1029432397f), sd2 = sp(lns.sd * lns.sd), nmean = sp(lns.nmean);
      const int us = (j / 3) * 16 + (j % 3) * 6, nu = (j % 3 == 2) ? 4 : 6;
      const int ho = H_OFF + pass * H_SZ + (tb * 86 + us) * 1024 + ln * 16;
#pragma unroll
      for (int u = 0; u < 6; ++u) {
        if (u < 4 || nu == 6) {
          unsigned hv[4];
#pragma unroll
          for (int g = 0; g < 2; ++g) {
            const f32x4 cg = *(LAS f32x4*)(L + CS3_OFF + (32 * u + 8 * g + 4 * h) * 4), cu = *(LAS f32x4*)(L + CS3_OFF + (32 * u + 16 + 8 * g + 4 * h) * 4);
#pragma unroll
            for (int i = 0; i < 2; ++i) {
              const f32x2 tg = fma2(f32x2{cg[2 * i], cg[2 * i + 1]}, nmean, f32x2{acc[u][4 * g + 2 * i], acc[u][4 * g + 2 * i + 1]});
              const f32x2 tu = fma2(f32x2{cu[2 * i], cu[2 * i + 1]}, nmean, f32x2{acc[u][8 + 4 * g + 2 * i], acc[u][8 + 4 * g + 2 * i + 1]});
              const f32x2 z = tg * fma2(tg * tg, k3, k1);
              const f32x2 d = fma2(f32x2{__builtin_amdgcn_exp2f(z.x), __builtin_amdgcn_exp2f(z.y)}, sd2, sd2);
              hv[2 * g + i] = pk(tg * tu * f32x2{__builtin_amdgcn_rcpf(d.x), __builtin_amdgcn_rcpf(d.y)});
            }
          }
          stx(v4u{hv[0], hv[1], hv[2], hv[3]}, rs, ho + u * 1024);
          if (u == 3) { asm volatile("s_waitcnt vmcnt(1)" ::: "memory"); sig<SY_PROG>(vsw, eb + 3); }
        }
        SBAR();
      }
      res = ld_resid(rs, ln, wave, t, j, xin + X_SZ);
      STAMP(18 + 4 * (ph + 2));
      hold<3>(vs, eb + 4);
      publish(vsw, eb + 4);
      STAMP(19 + 4 * (ph + 2));
    }
    { int ln = lane; asm volatile("" : "+v"(ln));
      proj_phase<43, true>(p, L, vs, rs, ln, wave, t, j, pass, have, H_OFF + pass * H_SZ, res, xin + 2 * X_SZ, stin + 2 * ST_SZ, eb + 3, ph + 3); }
  }
}

__global__ void __launch_bounds__(512, 2) fused_kernel(Params p) {
  extern __shared__ __attribute__((aligned(16))) char smem[];
  LAS char* L = (LAS char*)smem;
  const int lane = threadIdx.x & 63, wave = __builtin_amdgcn_readfirstlane(threadIdx.x >> 6);
  const int b = blockIdx.x;
  const int t = ((b & 7) >> 1) * 4 + (b >> 6), j = (b & 1) * 8 + ((b >> 3) & 7);
  asm volatile(".subsection 2\n\t.fill 128, 4, 0xBF800000\n\t.subsection 0");
  if (threadIdx.x < 44) *(LAS unsigned*)(L + SY_OFF + threadIdx.x * 4) = 0u;
  __syncthreads();
  if (wave == 7) {
    u64 pc; asm volatile("s_getpc_b64 %0" : "=s"(pc));
    const GAS char* c = (const GAS char*)(pc & ~255ull) + (b >> 3) * CODEPF + lane * 128;
    unsigned x = 0;
    if (lane * 128 < CODEPF) x = *(const GAS unsigned*)c;
    asm volatile("" :: "v"(x));
  }
  const unsigned lb = (unsigned)(size_t)L;
  unsigned vs = lb + SY_OFF; asm volatile("" : "+v"(vs));
  if (wave < 4) { __builtin_amdgcn_s_setprio(2); compute_main(p, L, vs, lane, wave, t, j); }
  else if (wave == 4) { __builtin_amdgcn_s_setprio(3); poller_main(p, L, vs, lane, t, j, wave); }
  else if (wave == 5) { __builtin_amdgcn_s_setprio(3); helper_main(p, L, lb, vs, lane, t, j); }
  else {
    loader_main(p, L, lb, lane, t, j, wave - 6, wave); }
  PROF_DUMP();
}
}

extern "C" void kernel_launch(void* const* d_in, const int* in_sizes, int n_in, void* d_out, int out_size, void* d_ws, size_t ws_size, hipStream_t stream) {
  Params p;
  p.x = (const u16*)d_in[0]; p.wqkv = (const u16*)d_in[1]; p.wo = (const u16*)d_in[2];
  p.wg = (const u16*)d_in[4]; p.wu = (const u16*)d_in[5]; p.wd = (const u16*)d_in[6];
  p.out = (u16*)d_out; p.ws = (unsigned char*)d_ws;
  (void)hipFuncSetAttribute((const void*)fused_kernel, hipFuncAttributeMaxDynamicSharedMemorySize, LDS_ALLOC);
  hipLaunchKernelGGL(fused_kernel, dim3(256), dim3(512), LDS_ALLOC, stream, p);
}
```
